```python
import torch

def _softmax(x, axis=-1):
    return torch.softmax(x, dim=axis)
def _gelu(x):
    return torch.nn.functional.gelu(x, approximate="tanh")
def _silu(x):
    return torch.nn.functional.silu(x)
def _sigmoid(x):
    return torch.sigmoid(x)
def _layernorm(x, axis=-1, eps=1e-5):
    m = x.mean(dim=axis, keepdim=True)
    v = x.var(dim=axis, keepdim=True, unbiased=False)
    return (x - m) / torch.sqrt(v + eps)
def _scatter_add(tgt, idx, src, axis=0):
    return tgt.clone().index_add_(axis, idx, src)
def _segment_sum(src, seg, n):
    out = torch.zeros((n,) + src.shape[1:], dtype=src.dtype)
    return out.index_add_(0, seg, src)
def _pad(x, pw):
    flat = tuple(v for pair in reversed(pw) for v in pair)
    return torch.nn.functional.pad(x, flat)

kernel_name = 'sequential_preLN__spectral_gfnet__dense_activation_squared_relu__prompt'

def setup_inputs():

    _w = (lambda a: (lambda t: t.to(torch.bfloat16) if t.is_floating_point() else t)(torch.as_tensor(a)))
    def _A_setup():
        B, S, D = 1, 2048, 512
        Df = D // 2 + 1
        return {
            "x": _w((lambda *s: torch.randn(*s, dtype=torch.bfloat16) * 0.05)(B, S, D)),
            "w_r": _w((lambda *s: torch.randn(*s, dtype=torch.bfloat16) * 0.4)(S, Df) * 0.02),
            "w_i": _w((lambda *s: torch.randn(*s, dtype=torch.bfloat16) * 0.4)(S, Df) * 0.02),
        }
    _A_in = _A_setup()

    def _B_setup():
        B, S, D, F = 1, 2048, 512, 1376
        return {
            "x": _w((lambda *s: torch.randn(*s, dtype=torch.bfloat16) * 0.05)(B, S, D)),
            "w1": _w((lambda *s: torch.randn(*s, dtype=torch.bfloat16) * 0.05)(D, F)),
            "b1": _w((lambda *s: torch.randn(*s, dtype=torch.bfloat16) * 0.2)(F)),
            "w2": _w((lambda *s: torch.randn(*s, dtype=torch.bfloat16) * 0.05)(F, D)),
            "b2": _w((lambda *s: torch.randn(*s, dtype=torch.bfloat16) * 0.8)(D)),
        }
    _B_in = _B_setup()

    return {
        "A_x": _A_in["x"],
        "A_w_r": _A_in["w_r"],
        "A_w_i": _A_in["w_i"],
        "B_x": _B_in["x"],
        "B_w1": _B_in["w1"],
        "B_b1": _B_in["b1"],
        "B_w2": _B_in["w2"],
        "B_b2": _B_in["b2"],
    }

def reference(A_x, A_w_r, A_w_i, B_x, B_w1, B_b1, B_w2, B_b2):
    def _A_ref(x, w_r, w_i):
        B, S, D = x.shape
        X = (lambda x, axes=(-2,-1): torch.fft.rfft2(x.float(), dim=axes))(x, axes=(-2, -1))
        W = w_r + 1j * w_i
        X = X * W
        y = (lambda x, s, axes=(-2,-1): torch.fft.irfft2(x, s=s, dim=axes).to(torch.bfloat16))(X, (S, D), axes=(-2, -1))
        return y

    def _B_ref(x, w1, b1, w2, b2):
        h = torch.einsum("bsd,df->bsf", x, w1) + b1
        zero = torch.zeros_like(h)
        r = torch.where(h > zero, h, zero)
        h = r * r
        out = torch.einsum("bsf,fd->bsd", h, w2) + b2
        return out

    x = A_x
    h = x + _A_ref(_layernorm(x, axis=-1), A_w_r, A_w_i)
    h2 = h + _B_ref(_layernorm(h, axis=-1), B_w1, B_b1, B_w2, B_b2)
    return h2
```

```cpp
#include <hip/hip_runtime.h>
#include <hip/hip_bf16.h>
#include <stdint.h>

#define SPLIT 1

typedef unsigned u32;
typedef u32 u32x2 __attribute__((ext_vector_type(2)));
typedef u32 u32x4 __attribute__((ext_vector_type(4)));
typedef float f32x4 __attribute__((ext_vector_type(4)));
typedef float f32x2 __attribute__((ext_vector_type(2)));
typedef float f32x16 __attribute__((ext_vector_type(16)));
typedef __bf16 bf16x8 __attribute__((ext_vector_type(8)));
typedef __bf16 bf16x2 __attribute__((ext_vector_type(2)));
typedef __amdgpu_buffer_rsrc_t rsrc_t;
#define LDS __attribute__((address_space(3)))
#define DEV __device__ __forceinline__

constexpr int S_ = 2048, D_ = 512, F_ = 1376, DF_ = 257;
constexpr u32 PST = 8448;
constexpr u32 O_X1 = 0, O_WT = 0x210000, O_X2 = 0x420000, O_W1 = 0x630000, O_W2 = 0x790000,
              O_CB = 0x900000, O_HB = 0x910000, O_FL = 0xF00000, O_HT = 0x1000000, WS_END = 0x1300000;
constexpr u32 POISON = 0xFFFFFFFFu;
constexpr int SC1 = 16;

DEV rsrc_t mkrs(const void* p, u32 bytes) { return __builtin_amdgcn_make_buffer_rsrc((void*)p, 0, bytes, 0x00020000); }
DEV u32 pk(float a, float b) { f32x2 v = {a, b}; return __builtin_bit_cast(u32, __builtin_convertvector(v, bf16x2)); }
DEV float blo(u32 v) { return __builtin_bit_cast(float, v << 16); }
DEV float bhi(u32 v) { return __builtin_bit_cast(float, v & 0xFFFF0000u); }
DEV float rbf(float a) { return blo(pk(a, a)); }
DEV float cosr(float rev) { return __builtin_amdgcn_cosf(rev); }
DEV float sinr(float rev) { return __builtin_amdgcn_sinf(rev); }
DEV f32x4 mm(u32x4 a, u32x4 b, f32x4 c) { return __builtin_amdgcn_mfma_f32_16x16x32_bf16(__builtin_bit_cast(bf16x8, a), __builtin_bit_cast(bf16x8, b), c, 0, 0, 0); }
DEV f32x16 mm32(u32x4 a, u32x4 b, f32x16 c) { return __builtin_amdgcn_mfma_f32_32x32x16_bf16(__builtin_bit_cast(bf16x8, a), __builtin_bit_cast(bf16x8, b), c, 0, 0, 0); }
template <int C> DEV float dpp(float x) { return __builtin_bit_cast(float, __builtin_amdgcn_mov_dpp(__builtin_bit_cast(int, x), C, 0xf, 0xf, true)); }
DEV float wsum(float v) {
  v += dpp<0xB1>(v); v += dpp<0x4E>(v); v += dpp<0x141>(v); v += dpp<0x128>(v);
  u32 u = __builtin_bit_cast(u32, v);
  auto a = __builtin_amdgcn_permlane16_swap(u, u, false, false);
  v = __builtin_bit_cast(float, (u32)a[0]) + __builtin_bit_cast(float, (u32)a[1]);
  u = __builtin_bit_cast(u32, v);
  auto b = __builtin_amdgcn_permlane32_swap(u, u, false, false);
  return __builtin_bit_cast(float, (u32)b[0]) + __builtin_bit_cast(float, (u32)b[1]);
}
DEV u32x4 ldsr(LDS char* p) { return *(LDS u32x4*)p; }
DEV void ldsw(LDS char* p, u32x4 v) { *(LDS u32x4*)p = v; }
DEV void bar() { asm volatile("s_waitcnt lgkmcnt(0)" ::: "memory"); __builtin_amdgcn_s_barrier(); asm volatile("" ::: "memory"); }
DEV u32x4 selfrag(int r, int h, int p) {
  u32x4 s;
#pragma unroll
  for (int i = 0; i < 4; ++i) { int e = 16 * p + r - 8 * h - 2 * i; s[i] = e == 0 ? 0x3F80u : (e == 1 ? 0x3F800000u : 0u); }
  return s;
}
DEV void lnstat(float s1, float s2, float& mb, float& inv) {
  float m = s1 * (1.f / 512.f);
  float v = fmaxf(s2 * (1.f / 512.f) - m * m, 0.f);
  mb = rbf(m);
  inv = __builtin_amdgcn_rcpf(rbf(__builtin_amdgcn_sqrtf(rbf(rbf(v) + 1e-5f))));
}
DEV int kd_of(int cb) { int l = cb >> 2, k1 = l & 15, k2 = 4 * (l >> 4) + (cb & 3); return k1 == 0 ? 16 * k2 : (k2 < 8 ? k1 + 32 * k2 : 512 - k1 - 32 * k2); }

__global__ __launch_bounds__(512) void k_pre(const uint16_t* __restrict__ x, const uint16_t* __restrict__ wr, const uint16_t* __restrict__ wi,
                                             const uint16_t* __restrict__ w1, const uint16_t* __restrict__ b1, const uint16_t* __restrict__ w2,
                                             char* __restrict__ ws) {
  __shared__ __attribute__((aligned(16))) char sm_[8 * 1040];
  LDS char* sm = (LDS char*)sm_;
  const int t = threadIdx.x, l = t & 63, wv = __builtin_amdgcn_readfirstlane(t >> 6), r = l & 15, h = l >> 4;
  const int bid = blockIdx.x;
  if (bid < 256) {
    const int s = 256 * (bid >> 5) + 32 * wv + (bid & 31);
    const u32x4 xf = *(const u32x4*)(x + s * D_ + 32 * r + 8 * h);
    const int ks0 = (bid & 63) + 64 * (8 * (bid >> 6) + 4 * (t & 1));
    const int kd = kd_of(t >> 1);
    u32 wa[4], wb[4], na[4], nb[4];
#pragma unroll
    for (int i = 0; i < 4; ++i) { wa[i] = wr[(ks0 + 64 * i) * DF_ + kd]; wb[i] = wi[(ks0 + 64 * i) * DF_ + kd]; }
    if (t < 2) {
#pragma unroll
      for (int i = 0; i < 4; ++i) { na[i] = wr[(ks0 + 64 * i) * DF_ + 256]; nb[i] = wi[(ks0 + 64 * i) * DF_ + 256]; }
    }
    { u32x4 p = {POISON, POISON, POISON, POISON}; *(u32x4*)(ws + O_X2 + bid * PST + t * 16) = p; }
    if (t == 0) *(u32*)(ws + O_FL + bid * 16) = 0;
    u32x4 C1, S1, R2, I2, SR, SI; float tc[4], ts[4];
#pragma unroll
    for (int i = 0; i < 4; ++i) {
      float a0 = (float)(((8 * h + 2 * i) * r) & 31) * (1.f / 32.f), a1 = (float)(((8 * h + 2 * i + 1) * r) & 31) * (1.f / 32.f);
      C1[i] = pk(cosr(a0), cosr(a1));
      S1[i] = r == 0 ? 0xBF803F80u : pk(-sinr(a0), -sinr(a1));
      const int n2 = 4 * h + i;
      float at = (float)(r * n2) * (1.f / 512.f); tc[i] = cosr(at); ts[i] = sinr(at);
      float ph = (float)((r * n2) & 15) * (1.f / 16.f), c = cosr(ph), sn = sinr(ph);
      R2[i] = pk(c, sn); I2[i] = pk(-sn, c) ^ (r >= 8 ? 0x80008000u : 0u);
      float ps = (float)((r * n2) & 31) * (1.f / 32.f);
      u32 cs = pk(cosr(ps), r == 0 ? ((i & 1) ? -1.f : 1.f) : -sinr(ps));
      SR[i] = (r & 1) ? cs << 16 : cs & 0xFFFFu;
      SI[i] = (r & 1) ? cs & 0xFFFF0000u : cs >> 16;
    }
    const f32x4 z4 = {0.f, 0.f, 0.f, 0.f};
    f32x4 d0 = mm(xf, selfrag(r, h, 0), z4), d1 = mm(xf, selfrag(r, h, 1), z4);
    float s1 = 0.f, s2 = 0.f;
#pragma unroll
    for (int j = 0; j < 4; ++j) { s1 += d0[j] + d1[j]; s2 = fmaf(d0[j], d0[j], fmaf(d1[j], d1[j], s2)); }
    s1 = wsum(s1); s2 = wsum(s2);
    float mb, inv; lnstat(s1, s2, mb, inv);
    u32x4 a;
#pragma unroll
    for (int j = 0; j < 4; ++j) a[j] = pk((d0[j] - mb) * inv, (d1[j] - mb) * inv);
    f32x4 yr = mm(a, C1, z4), yi = mm(a, S1, z4);
    u32x4 b;
#pragma unroll
    for (int j = 0; j < 4; ++j) b[j] = pk(yr[j] * tc[j] + yi[j] * ts[j], yi[j] * tc[j] - yr[j] * ts[j]);
    f32x4 xr = mm(R2, b, z4), xi = mm(I2, b, z4), sr = mm(SR, b, z4), si = mm(SI, b, z4);
    u32x4 o;
#pragma unroll
    for (int j = 0; j < 4; ++j) o[j] = pk(r == 0 ? sr[j] : xr[j], r == 0 ? si[j] : xi[j]);
    ldsw(sm + wv * 1040 + l * 16, o);
    { u32x4 q;
#pragma unroll
      for (int i = 0; i < 4; ++i) q[i] = wa[i] | (wb[i] << 16);
      *(u32x4*)(ws + O_WT + bid * PST + t * 16) = q;
      if (t < 2) {
#pragma unroll
        for (int i = 0; i < 4; ++i) q[i] = na[i] | (nb[i] << 16);
        *(u32x4*)(ws + O_WT + bid * PST + 8192 + t * 16) = q;
      } }
    bar();
    u32x4 q;
#pragma unroll
    for (int j = 0; j < 4; ++j) q[j] = *(LDS u32*)(sm + (4 * (t & 1) + j) * 1040 + (t >> 1) * 4);
    *(u32x4*)(ws + O_X1 + bid * PST + t * 16) = q;
  } else if (bid < 256 + 86) {
    const int ft = bid - 256, ks = t >> 5, hq = (t >> 3) & 3, fp = t & 7;
    u32 v[8];
#pragma unroll
    for (int j = 0; j < 8; ++j) v[j] = *(const u32*)(w1 + (32 * ks + 16 * (j >> 2) + 4 * hq + (j & 3)) * F_ + 16 * ft + 2 * fp);
    float bias = t < 16 ? blo(b1[16 * ft + (t & 15)]) : 0.f;
    u32x4 p0, p1; float c0 = 0.f, c1 = 0.f;
#pragma unroll
    for (int i = 0; i < 4; ++i) {
      p0[i] = __builtin_amdgcn_perm(v[2 * i + 1], v[2 * i], 0x05040100u); p1[i] = __builtin_amdgcn_perm(v[2 * i + 1], v[2 * i], 0x07060302u);
      c0 += blo(v[2 * i]) + blo(v[2 * i + 1]); c1 += bhi(v[2 * i]) + bhi(v[2 * i + 1]);
    }
    char* dst = ws + O_W1 + ft * 16384 + ks * 1024 + (hq * 16 + 2 * fp) * 16;
    *(u32x4*)dst = p0; *(u32x4*)(dst + 16) = p1;
    *(LDS f32x2*)(sm + t * 8) = f32x2{c0, c1};
    bar();
    if (t < 64) {
      float c = 0.f;
#pragma unroll
      for (int i = 0; i < 16; ++i) c += *(LDS float*)(sm + ((h * 16 + i) * 16 + r) * 4);
      u32 u = __builtin_bit_cast(u32, c);
      auto a = __builtin_amdgcn_permlane16_swap(u, u, false, false);
      c = __builtin_bit_cast(float, (u32)a[0]) + __builtin_bit_cast(float, (u32)a[1]);
      u = __builtin_bit_cast(u32, c);
      auto b = __builtin_amdgcn_permlane32_swap(u, u, false, false);
      c = __builtin_bit_cast(float, (u32)b[0]) + __builtin_bit_cast(float, (u32)b[1]);
      if (t < 16) { float* cbp = (float*)(ws + O_CB + ft * 128 + (t >> 2) * 32); cbp[t & 3] = c; cbp[4 + (t & 3)] = bias; }
    }
  } else {
    const int kt = bid - 256 - 86, hh = t >> 8, dp = t & 255;
    u32 v[8];
#pragma unroll
    for (int j = 0; j < 8; ++j) v[j] = *(const u32*)(w2 + (16 * kt + 8 * hh + j) * D_ + 2 * dp);
    u32x4 p0, p1;
#pragma unroll
    for (int i = 0; i < 4; ++i) { p0[i] = __builtin_amdgcn_perm(v[2 * i + 1], v[2 * i], 0x05040100u); p1[i] = __builtin_amdgcn_perm(v[2 * i + 1], v[2 * i], 0x07060302u); }
    char* dst = ws + O_W2 + ((dp >> 4) * 86 + kt) * 1024 + (hh * 32 + 2 * (dp & 15)) * 16;
    *(u32x4*)dst = p0; *(u32x4*)(dst + 16) = p1;
  }
}

constexpr int L_HT = 0;
constexpr int L_ST = 33280;
constexpr int L_H = 33536;
constexpr int L_B0 = L_H, L_B1 = L_H + 8192, L_C0 = L_H + 24576;
constexpr int L_RED = L_H + 88 * 1024;
constexpr int L_END = L_RED + 2 * 32 * 528;

template <int PH>
__global__ __launch_bounds__(512, 2) void k_main(const uint16_t* __restrict__ x, const uint16_t* __restrict__ b2, char* __restrict__ ws, uint16_t* __restrict__ out) {
  extern __shared__ __attribute__((aligned(16))) char smem_[];
  LDS char* sm = (LDS char*)smem_;
  const int t = threadIdx.x, l = t & 63, wv = __builtin_amdgcn_readfirstlane(t >> 6), r = l & 15, h = l >> 4;
  const int cb = (blockIdx.x & 7) * 32 + (blockIdx.x >> 3), g = cb >> 2, jb = cb & 3;
  const rsrc_t rw = mkrs(ws, WS_END);
  const f32x4 z4 = {0.f, 0.f, 0.f, 0.f};
  const int srow0 = 512 * (g >> 4) + 256 * (g & 1) + 16 * ((g >> 3) & 1) + 4 * ((g >> 1) & 3);

  if (PH & 1) {
    const int w = wv & 3, job = wv >> 2;
    const bool act = job == 0 || cb == 0;
    const u32x4 xin = __builtin_amdgcn_raw_buffer_load_b128(rw, O_X1 + (t >> 1) * PST + cb * 32 + (t & 1) * 16, 0, 0);
    u32x4 wt[2], R1[4], I1[4], R2[2][2], I2[2][2];
    float tc[2][4], ts[2][4], uc[2][4], us[2][4];
    if (act) {
#pragma unroll
      for (int u = 0; u < 2; ++u)
        wt[u] = __builtin_amdgcn_raw_buffer_load_b128(rw, O_WT + ((2 * u + (h >> 1)) * 64 + 16 * w + r) * PST + (job ? 8192 : cb * 32) + (h & 1) * 16, 0, 0);
      const int k1 = 16 * w + r;
      const u32 odd = (r & 1) ? 0x80008000u : 0u, two = (r & 2) ? 0x80008000u : 0u;
#pragma unroll
      for (int i = 0; i < 4; ++i) {
        const int m = 4 * h + i;
        float a = (float)((k1 * m) & 63) * (1.f / 64.f), c = cosr(a), sn = sinr(a);
        u32 R = pk(c, sn), I = pk(-sn, c);
        R1[0][i] = R; I1[0][i] = I; R1[2][i] = R ^ odd; I1[2][i] = I ^ odd;
        u32 Rq = ((r & 1) ? I : R) ^ two, Iq = ((r & 1) ? R : I) ^ two ^ odd;
        R1[1][i] = Rq; I1[1][i] = Iq; R1[3][i] = Rq ^ odd; I1[3][i] = Iq ^ odd;
        a = (float)((r * m) & 31) * (1.f / 32.f); c = cosr(a); sn = sinr(a);
        R = pk(c, sn); I = pk(-sn, c);
        const u32 fi = (i & 1) ? 0x80008000u : 0u;
        R2[0][0][i] = R; I2[0][0][i] = I; R2[0][1][i] = R ^ odd; I2[0][1][i] = I ^ odd;
        R2[1][0][i] = R ^ fi; I2[1][0][i] = I ^ fi; R2[1][1][i] = R ^ odd ^ fi; I2[1][1][i] = I ^ odd ^ fi;
#pragma unroll
        for (int u = 0; u < 2; ++u) {
          a = (float)(k1 * (16 * u + m)) * (1.f / 2048.f); tc[u][i] = cosr(a) * 0x1p-20f; ts[u][i] = sinr(a) * 0x1p-20f;
          a = (float)((16 * w + m) * (16 * u + r)) * (1.f / 2048.f); uc[u][i] = cosr(a); us[u][i] = sinr(a);
        }
      }
    }
    ldsw(sm + L_B0 + t * 16, xin);
    bar();
    if (act) {
      u32x4 bf[2];
#pragma unroll
      for (int u = 0; u < 2; ++u) {
        f32x4 yr = z4, yi = z4;
#pragma unroll
        for (int q = 0; q < 4; ++q) {
          u32x4 a = ldsr(sm + L_B0 + ((2 * q + (h >> 1)) * 32 + 16 * u + r) * 32 + (h & 1) * 16);
          if (cb == 0) {
#pragma unroll
            for (int i = 0; i < 4; ++i) a[i] = job ? a[i] >> 16 : a[i] & 0xFFFFu;
          }
          yr = mm(a, R1[q], yr); yi = mm(a, I1[q], yi);
        }
#pragma unroll
        for (int j = 0; j < 4; ++j) bf[u][j] = pk(yr[j] * tc[u][j] + yi[j] * ts[u][j], yi[j] * tc[u][j] - yr[j] * ts[u][j]);
      }
      u32x4 ef[2];
#pragma unroll
      for (int u = 0; u < 2; ++u) {
        f32x4 xr = mm(R2[u][1], bf[1], mm(R2[u][0], bf[0], z4)), xi = mm(I2[u][1], bf[1], mm(I2[u][0], bf[0], z4));
#pragma unroll
        for (int j = 0; j < 4; ++j) { float a = blo(wt[u][j]), b = bhi(wt[u][j]); ef[u][j] = pk(xr[j] * a - xi[j] * b, -(xr[j] * b + xi[j] * a)); }
      }
#pragma unroll
      for (int u = 0; u < 2; ++u) {
        f32x4 yr = mm(ef[1], R2[u][1], mm(ef[0], R2[u][0], z4)), yi = mm(ef[1], I2[u][1], mm(ef[0], I2[u][0], z4));
        u32x4 o;
#pragma unroll
        for (int j = 0; j < 4; ++j) o[j] = pk(yr[j] * uc[u][j] + yi[j] * us[u][j], yi[j] * uc[u][j] - yr[j] * us[u][j]);
        ldsw(sm + L_B1 + job * 8192 + ((u * 4 + w) * 64 + l) * 16, o);
      }
    }
    bar();
    if (job == 0) {
#pragma unroll
      for (int u = 0; u < 2; ++u) {
        f32x4 zr = z4, zi = z4;
        if (cb != 0) {
#pragma unroll
          for (int q = 0; q < 4; ++q) { u32x4 a = ldsr(sm + L_B1 + ((u * 4 + q) * 64 + l) * 16); zr = mm(a, R1[q], zr); zi = mm(a, I1[q], zi); }
          zi = -zi;
        } else {
#pragma unroll
          for (int q = 0; q < 4; ++q) {
            zr = mm(ldsr(sm + L_B1 + ((u * 4 + q) * 64 + l) * 16), R1[q], zr);
            zi = mm(ldsr(sm + L_B1 + 8192 + ((u * 4 + q) * 64 + l) * 16), R1[q], zi);
          }
        }
        u32x4 o;
#pragma unroll
        for (int j = 0; j < 4; ++j) { u32 v = pk(zr[j], zi[j]); o[j] = v == POISON ? 0x7FC07FC0u : v; }
        __builtin_amdgcn_raw_buffer_store_b128(o, rw, O_X2 + cb * PST + ((w * 2 + u) * 64 + l) * 16, 0, SC1);
      }
    }
  }

  if (PH & 2) {
    u32x4 zin[4], xf[4];
#pragma unroll
    for (int i = 0; i < 4; ++i) zin[i] = __builtin_amdgcn_raw_buffer_load_b128(rw, O_X2 + (i * 64 + (t >> 3)) * PST + g * 128 + (t & 7) * 16, 0, SC1);
#pragma unroll
    for (int i = 0; i < 4; ++i) xf[i] = *(const u32x4*)(x + (srow0 + 32 * wv + i) * D_ + 32 * r + 8 * h);
    u32x4 wf[3][16];
    const int ft0 = 22 * jb + wv;
    const bool has3 = wv < 6 && ft0 + 16 < 86;
#pragma unroll
    for (int ks = 0; ks < 16; ++ks) {
#pragma unroll
      for (int c = 0; c < 2; ++c) wf[c][ks] = __builtin_amdgcn_raw_buffer_load_b128(rw, l * 16 + ks * 1024, O_W1 + (ft0 + 8 * c) * 16384, 0);
    }
    u32x4 R, I, SA, SB, M0, M1; float tc[4], ts[4];
#pragma unroll
    for (int i = 0; i < 4; ++i) {
      const int m = 4 * h + i;
      float a = (float)((r * m) & 15) * (1.f / 16.f), c = cosr(a), sn = sinr(a);
      const u32 sg = h >= 2 ? 0x80000000u : 0u;
      R[i] = pk(c, -sn) ^ sg; I[i] = pk(sn, c) ^ sg;
      a = (float)((r * m) & 31) * (1.f / 32.f);
      u32 v = pk(2.f * cosr(a), -2.f * sinr(a));
      if (i == 0) v = h == 0 ? ((r & 1) ? 0xBF803F80u : 0x3F803F80u) : v;
      SA[i] = (i & 1) ? 0u : v; SB[i] = (i & 1) ? v : 0u;
      a = (float)(r * m) * (1.f / 512.f); tc[i] = cosr(a); ts[i] = sinr(a);
      M0[i] = pk(2.f * c, -2.f * sn);
      a = (float)((m * (2 * r + 1)) & 31) * (1.f / 32.f);
      M1[i] = pk(2.f * cosr(a), -2.f * sinr(a));
      if (i == 0) { M0[i] = h == 0 ? 0x3F803F80u : M0[i]; M1[i] = h == 0 ? 0xBF803F80u : M1[i]; }
    }
    const u32x4 E0 = selfrag(r, h, 0), E1 = selfrag(r, h, 1);
#pragma unroll
    for (int i = 0; i < 4; ++i) { const int c = i * 64 + (t >> 3); ldsw(sm + L_C0 + (t & 7) * 4112 + (c & 3) * 1024 + (c >> 2) * 16, zin[i]); }
    bar();
    {
      u32x4 q[4];
#pragma unroll
      for (int j = 0; j < 4; ++j) q[j] = ldsr(sm + L_C0 + wv * 4112 + j * 1024 + l * 16);
#pragma unroll
      for (int i = 0; i < 4; ++i) {
        const u32x4 z = {q[0][i], q[1][i], q[2][i], q[3][i]};
        f32x4 ur = mm(z, R, z4), ui = mm(z, I, z4), sa = mm(z, SA, z4), sb = mm(z, SB, z4);
        f32x4 y0 = mm(E0, xf[i], z4), y1 = mm(E1, xf[i], z4);
        ur[0] = h == 0 ? sa[0] : ur[0]; ui[0] = h == 0 ? sb[0] : ui[0];
        u32x4 v;
#pragma unroll
        for (int j = 0; j < 4; ++j) v[j] = pk(ur[j] * tc[j] - ui[j] * ts[j], ui[j] * tc[j] + ur[j] * ts[j]);
        y0 = mm(v, M0, y0); y1 = mm(v, M1, y1);
        const u32x4 o = {pk(y0[0], y0[1]), pk(y0[2], y0[3]), pk(y1[0], y1[1]), pk(y1[2], y1[3])};
        ldsw(sm + L_HT + (4 * wv + i) * 1040 + l * 16, o);
        if (PH != 7 && jb == 0) __builtin_amdgcn_raw_buffer_store_b128(o, rw, O_HT + g * 33280 + (4 * wv + i) * 1040 + l * 16, 0, 0);
      }
    }
    if (has3) {
#pragma unroll
      for (int ks = 0; ks < 16; ++ks) wf[2][ks] = __builtin_amdgcn_raw_buffer_load_b128(rw, l * 16 + ks * 1024, O_W1 + (ft0 + 16) * 16384, 0);
    }
    bar();
    f32x4 acc[3][2];
#pragma unroll
    for (int c = 0; c < 3; ++c) { acc[c][0] = z4; acc[c][1] = z4; }
    const u32x4 ones = {0x3F803F80u, 0x3F803F80u, 0x3F803F80u, 0x3F803F80u};
    if (wv < 6) {
#pragma unroll
      for (int ks = 0; ks < 16; ++ks) {
        const u32x4 a0 = ldsr(sm + L_HT + r * 1040 + (h * 16 + ks) * 16), a1 = ldsr(sm + L_HT + (16 + r) * 1040 + (h * 16 + ks) * 16);
#pragma unroll
        for (int c = 0; c < 3; ++c) { acc[c][0] = mm(wf[c][ks], a0, acc[c][0]); acc[c][1] = mm(wf[c][ks], a1, acc[c][1]); }
      }
    } else {
#pragma unroll
      for (int ks = 0; ks < 16; ++ks) {
        const u32x4 a0 = ldsr(sm + L_HT + r * 1040 + (h * 16 + ks) * 16), a1 = ldsr(sm + L_HT + (16 + r) * 1040 + (h * 16 + ks) * 16);
#pragma unroll
        for (int c = 0; c < 2; ++c) { acc[c][0] = mm(wf[c][ks], a0, acc[c][0]); acc[c][1] = mm(wf[c][ks], a1, acc[c][1]); }
        const u32x4 as = wv == 6 ? a0 : a1;
        acc[2][0] = mm(as, as, acc[2][0]); acc[2][1] = mm(ones, as, acc[2][1]);
      }
      const int e = r & 3;
      const float s2 = e == 0 ? acc[2][0][0] : e == 1 ? acc[2][0][1] : e == 2 ? acc[2][0][2] : acc[2][0][3];
      float mb, inv; lnstat(acc[2][1][0], s2, mb, inv);
      if (h == (r >> 2)) *(LDS f32x2*)(sm + L_ST + ((wv - 6) * 16 + r) * 8) = f32x2{mb, inv};
    }
    bar();
    const f32x2 st0 = *(LDS f32x2*)(sm + L_ST + r * 8), st1 = *(LDS f32x2*)(sm + L_ST + (16 + r) * 8);
#pragma unroll
    for (int c = 0; c < 3; ++c) {
      if (c < 2 || has3) {
        const int ft = ft0 + 8 * c;
        const f32x4 cs = __builtin_bit_cast(f32x4, __builtin_amdgcn_raw_buffer_load_b128(rw, h * 32, O_CB + ft * 128, 0));
        const f32x4 bb = __builtin_bit_cast(f32x4, __builtin_amdgcn_raw_buffer_load_b128(rw, h * 32 + 16, O_CB + ft * 128, 0));
        float v0[4], v1[4];
#pragma unroll
        for (int j = 0; j < 4; ++j) {
          float p0 = fmaxf((acc[c][0][j] - st0[0] * cs[j]) * st0[1] + bb[j], 0.f), p1 = fmaxf((acc[c][1][j] - st1[0] * cs[j]) * st1[1] + bb[j], 0.f);
          v0[j] = p0 * p0; v1[j] = p1 * p1;
        }
        auto s0 = __builtin_amdgcn_permlane16_swap(pk(v0[0], v0[1]), pk(v1[0], v1[1]), false, false);
        auto s1 = __builtin_amdgcn_permlane16_swap(pk(v0[2], v0[3]), pk(v1[2], v1[3]), false, false);
        const u32x4 o = {(u32)s0[0], (u32)s1[0], (u32)s0[1], (u32)s1[1]};
        __builtin_amdgcn_raw_buffer_store_b128(o, rw, l * 16, O_HB + (g * 88 + ft) * 1024, SC1);
        if (PH == 7) ldsw(sm + L_H + ft * 1024 + l * 16, o);
      }
    }
  }

  if (PH & 4) {
    const int cg = wv & 3, kh = wv >> 2;
    if (PH != 7) {
      for (int i = wv; i < 86; i += 8)
        __builtin_amdgcn_raw_ptr_buffer_load_lds(rw, (LDS void*)(sm + L_H + i * 1024), 16, l * 16, O_HB + (g * 88 + i) * 1024, 0, SC1);
      for (int i = wv; i < 33; i += 8)
        if (i < 32 || l < 32) __builtin_amdgcn_raw_ptr_buffer_load_lds(rw, (LDS void*)(sm + L_HT + i * 1024), 16, l * 16, O_HT + g * 33280 + i * 1024, 0, 0);
    }
    u32x4 w2f[43];
#pragma unroll
    for (int i = 0; i < 43; ++i) { int n = i + 11 * jb; n = n >= 43 ? n - 43 : n; w2f[i] = __builtin_amdgcn_raw_buffer_load_b128(rw, l * 16, O_W2 + ((4 * jb + cg) * 86 + 2 * n + kh) * 1024, 0); }
    const u32x4 bq = *(const u32x4*)(b2 + 128 * jb + 8 * (t & 15));
    asm volatile("s_waitcnt vmcnt(44)" ::: "memory");
    bar();
    f32x16 acc = {0.f};
#pragma unroll
    for (int i = 0; i < 43; ++i) { int n = i + 11 * jb; n = n >= 43 ? n - 43 : n; acc = mm32(w2f[i], ldsr(sm + L_H + (2 * n + kh) * 1024 + l * 16), acc); }
#pragma unroll
    for (int q = 0; q < 4; ++q)
      *(LDS f32x4*)(sm + L_RED + (kh * 32 + (l & 31)) * 528 + (32 * cg + 8 * q + 4 * (l >> 5)) * 4) = f32x4{acc[4 * q], acc[4 * q + 1], acc[4 * q + 2], acc[4 * q + 3]};
    bar();
    const int tok = t >> 4, e = t & 15, d0 = 128 * jb + 8 * e;
    LDS char* rp = sm + L_RED + tok * 528 + e * 32;
    const f32x4 a0 = *(LDS f32x4*)rp + *(LDS f32x4*)(rp + 32 * 528), a1 = *(LDS f32x4*)(rp + 16) + *(LDS f32x4*)(rp + 32 * 528 + 16);
    LDS char* hp = sm + L_HT + tok * 1040 + (((d0 >> 2) & 3) * 16 + (d0 >> 5)) * 16 + ((d0 >> 4) & 1) * 8;
    const u32x2 h0 = *(LDS u32x2*)hp, h1 = *(LDS u32x2*)(hp + 256);
    u32x4 o;
    o[0] = pk(a0[0] + blo(bq[0]) + blo(h0[0]), a0[1] + bhi(bq[0]) + bhi(h0[0]));
    o[1] = pk(a0[2] + blo(bq[1]) + blo(h0[1]), a0[3] + bhi(bq[1]) + bhi(h0[1]));
    o[2] = pk(a1[0] + blo(bq[2]) + blo(h1[0]), a1[1] + bhi(bq[2]) + bhi(h1[0]));
    o[3] = pk(a1[2] + blo(bq[3]) + blo(h1[1]), a1[3] + bhi(bq[3]) + bhi(h1[1]));
    *(u32x4*)(out + (srow0 + 32 * (tok >> 2) + (tok & 3)) * D_ + d0) = o;
  }
}

extern "C" void kernel_launch(void* const* d_in, const int* in_sizes, int n_in, void* d_out, int out_size, void* d_ws, size_t ws_size, hipStream_t stream) {
  const uint16_t* x = (const uint16_t*)d_in[0];
  k_pre<<<256 + 172, 512, 0, stream>>>(x, (const uint16_t*)d_in[1], (const uint16_t*)d_in[2], (const uint16_t*)d_in[4], (const uint16_t*)d_in[5], (const uint16_t*)d_in[6], (char*)d_ws);
  (void)hipFuncSetAttribute((const void*)k_main<1>, hipFuncAttributeMaxDynamicSharedMemorySize, L_END);
  (void)hipFuncSetAttribute((const void*)k_main<2>, hipFuncAttributeMaxDynamicSharedMemorySize, L_END);
  (void)hipFuncSetAttribute((const void*)k_main<4>, hipFuncAttributeMaxDynamicSharedMemorySize, L_END);
  k_main<1><<<256, 512, L_END, stream>>>(x, (const uint16_t*)d_in[7], (char*)d_ws, (uint16_t*)d_out);
  k_main<2><<<256, 512, L_END, stream>>>(x, (const uint16_t*)d_in[7], (char*)d_ws, (uint16_t*)d_out);
  k_main<4><<<256, 512, L_END, stream>>>(x, (const uint16_t*)d_in[7], (char*)d_ws, (uint16_t*)d_out);
}
```

```cpp
#define HPOIS 0
#define X1BAR 0
#define NDMA1 16
#define NW1A 8
#define NW2A 10
#define NW2B 18
#include <hip/hip_runtime.h>
#include <hip/hip_bf16.h>
#include <stdint.h>

typedef unsigned u32;
typedef u32 u32x2 __attribute__((ext_vector_type(2)));
typedef u32 u32x4 __attribute__((ext_vector_type(4)));
typedef float f32x4 __attribute__((ext_vector_type(4)));
typedef float f32x2 __attribute__((ext_vector_type(2)));
typedef float f32x16 __attribute__((ext_vector_type(16)));
typedef __bf16 bf16x8 __attribute__((ext_vector_type(8)));
typedef __bf16 bf16x2 __attribute__((ext_vector_type(2)));
typedef __amdgpu_buffer_rsrc_t rsrc_t;
#define LDS __attribute__((address_space(3)))
#define DEV __device__ __forceinline__

constexpr int S_ = 2048, D_ = 512, F_ = 1376, DF_ = 257;
constexpr u32 PST = 8448, PS1 = 12544;
constexpr u32 O_X1 = 0, O_WT = 0x320000, O_X2 = 0x540000, O_W1 = 0x760000, O_W2 = 0x8C0000,
              O_CB = 0xA20000, O_HB = 0xA40000, O_EP = 0x1000000, WS_END = 0x1001000;
constexpr u32 POISON = 0xFFFFFFFFu;
constexpr int SC1 = 16;
constexpr int SPIN = 1 << 15;
#define ST(i)
#define SB() __builtin_amdgcn_sched_barrier(0)
#define WAITV(n) asm volatile("s_waitcnt vmcnt(" #n ")" ::: "memory")

DEV rsrc_t mkrs(const void* p, u32 bytes) { return __builtin_amdgcn_make_buffer_rsrc((void*)p, 0, bytes, 0x00020000); }
DEV u32 pk(float a, float b) { f32x2 v = {a, b}; return __builtin_bit_cast(u32, __builtin_convertvector(v, bf16x2)); }
DEV float blo(u32 v) { return __builtin_bit_cast(float, v << 16); }
DEV float bhi(u32 v) { return __builtin_bit_cast(float, v & 0xFFFF0000u); }
DEV float rbf(float a) { return blo(pk(a, a)); }
DEV float cosr(float rev) { return __builtin_amdgcn_cosf(rev); }
DEV float sinr(float rev) { return __builtin_amdgcn_sinf(rev); }
DEV f32x4 mm(u32x4 a, u32x4 b, f32x4 c) { return __builtin_amdgcn_mfma_f32_16x16x32_bf16(__builtin_bit_cast(bf16x8, a), __builtin_bit_cast(bf16x8, b), c, 0, 0, 0); }
DEV f32x16 mm32(u32x4 a, u32x4 b, f32x16 c) { return __builtin_amdgcn_mfma_f32_32x32x16_bf16(__builtin_bit_cast(bf16x8, a), __builtin_bit_cast(bf16x8, b), c, 0, 0, 0); }
template <int C> DEV float dpp(float x) { return __builtin_bit_cast(float, __builtin_amdgcn_mov_dpp(__builtin_bit_cast(int, x), C, 0xf, 0xf, true)); }
DEV float wsum(float v) {
  v += dpp<0xB1>(v); v += dpp<0x4E>(v); v += dpp<0x141>(v); v += dpp<0x128>(v);
  u32 u = __builtin_bit_cast(u32, v);
  auto a = __builtin_amdgcn_permlane16_swap(u, u, false, false);
  v = __builtin_bit_cast(float, (u32)a[0]) + __builtin_bit_cast(float, (u32)a[1]);
  u = __builtin_bit_cast(u32, v);
  auto b = __builtin_amdgcn_permlane32_swap(u, u, false, false);
  return __builtin_bit_cast(float, (u32)b[0]) + __builtin_bit_cast(float, (u32)b[1]);
}
DEV u32x4 ldsr(LDS char* p) { return *(LDS u32x4*)p; }
DEV void ldsw(LDS char* p, u32x4 v) { *(LDS u32x4*)p = v; }
DEV void bar() { asm volatile("s_waitcnt lgkmcnt(0)" ::: "memory"); __builtin_amdgcn_s_barrier(); asm volatile("" ::: "memory"); }
DEV u32x4 selfrag(int r, int h, int p) {
  u32x4 s;
#pragma unroll
  for (int i = 0; i < 4; ++i) { int e = 16 * p + r - 8 * h - 2 * i; s[i] = e == 0 ? 0x3F80u : (e == 1 ? 0x3F800000u : 0u); }
  return s;
}
DEV void lnstat(float s1, float s2, float& mb, float& inv) {
  float m = s1 * (1.f / 512.f);
  float v = fmaxf(s2 * (1.f / 512.f) - m * m, 0.f);
  mb = rbf(m);
  inv = __builtin_amdgcn_rcpf(rbf(__builtin_amdgcn_sqrtf(rbf(rbf(v) + 1e-5f))));
}
DEV int kd_of(int cb) { int l = cb >> 2, k1 = l & 15, k2 = 4 * (l >> 4) + (cb & 3); return k1 == 0 ? 16 * k2 : (k2 < 8 ? k1 + 32 * k2 : 512 - k1 - 32 * k2); }

DEV float half_sum(float c) {
  u32 u = __builtin_bit_cast(u32, c);
  auto a = __builtin_amdgcn_permlane16_swap(u, u, false, false);
  c = __builtin_bit_cast(float, (u32)a[0]) + __builtin_bit_cast(float, (u32)a[1]);
  u = __builtin_bit_cast(u32, c);
  auto b = __builtin_amdgcn_permlane32_swap(u, u, false, false);
  return __builtin_bit_cast(float, (u32)b[0]) + __builtin_bit_cast(float, (u32)b[1]);
}
DEV u32x4 bld(rsrc_t w, u32 v, u32 s) { return __builtin_amdgcn_raw_buffer_load_b128(w, v, s, SC1); }
DEV void bst(u32x4 d, rsrc_t w, u32 v, u32 s) { __builtin_amdgcn_raw_buffer_store_b128(d, w, v, s, SC1); }
DEV void dma(rsrc_t w, u32 lds, u32 v, u32 s) { asm volatile("s_mov_b32 m0, %0\n\tbuffer_load_dwordx4 %1, %2, %3 offen sc1 lds" :: "s"(lds), "v"(v), "s"(w), "s"(s) : "memory"); }
DEV bool clean(u32x4 a) { return max(max(a[0], a[1]), max(a[2], a[3])) != POISON; }

constexpr int L_H = 0;
constexpr int L_RED = 90112;
constexpr int L_B0 = 98304, L_B1 = L_B0 + 8192;
constexpr int L_HT = 123904;
constexpr int L_ST = L_HT + 33280;
constexpr int L_CS = L_ST + 256, L_BB = L_CS + 1408;
constexpr int L_END = L_BB + 1408;

__global__ __launch_bounds__(512, 2) void k_all(const uint16_t* __restrict__ x, const uint16_t* __restrict__ wr, const uint16_t* __restrict__ wi,
                                                const uint16_t* __restrict__ w1, const uint16_t* __restrict__ b1, const uint16_t* __restrict__ w2,
                                                const uint16_t* __restrict__ b2, char* __restrict__ ws, uint16_t* __restrict__ out) {
  extern __shared__ __attribute__((aligned(16))) char smem_[];
  LDS char* sm = (LDS char*)smem_;
  ST(0);
  const int t = threadIdx.x, l = t & 63, wv = __builtin_amdgcn_readfirstlane(t >> 6), r = l & 15, h = l >> 4;
  const int bid = blockIdx.x;
  const int cb = (bid & 7) * 32 + (bid >> 3), g = cb >> 2, jb = cb & 3;
  const rsrc_t rw = mkrs(ws, WS_END);
  const f32x4 z4 = {0.f, 0.f, 0.f, 0.f};
  const u32x4 P4 = {POISON, POISON, POISON, POISON};
  const int srow0 = 512 * (g >> 4) + 256 * (g & 1) + 16 * ((g >> 3) & 1) + 4 * ((g >> 1) & 3);
  const int ft0 = 22 * jb + wv;
  const bool has3 = wv < 6 && ft0 + 16 < 86;
  const u32 tag = *(const __attribute__((address_space(4))) u32*)(ws + O_EP) + 1u;
  const int i0q = (t * 21846) >> 16, i0p = t - 3 * i0q, i1q = ((t + 512) * 21846) >> 16, i1p = t + 512 - 3 * i1q;
  u32x4 xf[4];

  {
    const u32x4 xa = *(const u32x4*)(x + (256 * (bid >> 5) + 32 * wv + (bid & 31)) * D_ + 32 * r + 8 * h);
    const int ks0 = (bid & 63) + 64 * (8 * (bid >> 6) + 4 * (t & 1));
    const int kd = kd_of(t >> 1);
    u32 wa[4], wb[4], na[4], nb[4], v[8];
#pragma unroll
    for (int i = 0; i < 4; ++i) { wa[i] = wr[(ks0 + 64 * i) * DF_ + kd]; wb[i] = wi[(ks0 + 64 * i) * DF_ + kd]; }
    const int id = (wv < 3 ? wv : wv - 3) * 256 + bid;
    const bool t1 = wv < 3 && id < 688, t2 = wv >= 3 && id < 688;
    if (t1) {
#pragma unroll
      for (int j = 0; j < 8; ++j) v[j] = *(const u32*)(w1 + (32 * (id & 15) + 16 * (j >> 2) + 4 * h + (j & 3)) * F_ + 32 * (id >> 4) + 2 * r);
    } else if (t2) {
#pragma unroll
      for (int j = 0; j < 8; ++j) v[j] = *(const u32*)(w2 + (32 * (id >> 4) + 16 * (l >> 5) + 2 * j + ((id >> 3) & 1)) * D_ + 64 * (id & 7) + 2 * (l & 31));
    } else if (wv == 7) {
#pragma unroll
      for (int i = 0; i < 4; ++i) { na[i] = wr[(ks0 + 64 * i) * DF_ + 256]; nb[i] = wi[(ks0 + 64 * i) * DF_ + 256]; }
    }
    const int fb = 22 * jb + (t >> 4);
    const u32 bv = b1[min(32 * (fb >> 1) + 2 * r + (fb & 1), F_ - 1)];
    bst(P4, rw, t * 16, O_X2 + cb * PST);
    SB();
    u32x4 C1, S1, R2, I2, SR, SI; float tc[4], ts[4];
#pragma unroll
    for (int i = 0; i < 4; ++i) {
      float a0 = (float)(((8 * h + 2 * i) * r) & 31) * (1.f / 32.f), a1 = (float)(((8 * h + 2 * i + 1) * r) & 31) * (1.f / 32.f);
      C1[i] = pk(cosr(a0), cosr(a1));
      S1[i] = r == 0 ? 0xBF803F80u : pk(-sinr(a0), -sinr(a1));
      const int n2 = 4 * h + i;
      float at = (float)(r * n2) * (1.f / 512.f); tc[i] = cosr(at); ts[i] = sinr(at);
      float ph = (float)((r * n2) & 15) * (1.f / 16.f), c = cosr(ph), sn = sinr(ph);
      R2[i] = pk(c, sn); I2[i] = pk(-sn, c) ^ (r >= 8 ? 0x80008000u : 0u);
      float ps = (float)((r * n2) & 31) * (1.f / 32.f);
      u32 cs = pk(cosr(ps), r == 0 ? ((i & 1) ? -1.f : 1.f) : -sinr(ps));
      SR[i] = (r & 1) ? cs << 16 : cs & 0xFFFFu;
      SI[i] = (r & 1) ? cs & 0xFFFF0000u : cs >> 16;
    }
    const u32x4 E0 = selfrag(r, h, 0), E1 = selfrag(r, h, 1);
    SB();
    ST(1);
    f32x4 d0 = mm(xa, E0, z4), d1 = mm(xa, E1, z4);
    { u32x4 q;
#pragma unroll
      for (int i = 0; i < 4; ++i) q[i] = wa[i] | (wb[i] << 16);
      bst(q, rw, t * 16, O_WT + bid * PST); }
    u32x4 p0, p1;
#pragma unroll
    for (int i = 0; i < 4; ++i) { p0[i] = __builtin_amdgcn_perm(v[2 * i + 1], v[2 * i], 0x05040100u); p1[i] = __builtin_amdgcn_perm(v[2 * i + 1], v[2 * i], 0x07060302u); }
    if (t1) {
      const u32 o = O_W1 + ((id >> 4) * 32 + (id & 15)) * 1024;
      bst(p0, rw, l * 16, o); bst(p1, rw, l * 16, o + 16384);
      float c0 = 0.f, c1 = 0.f;
#pragma unroll
      for (int j = 0; j < 8; ++j) { c0 += blo(v[j]); c1 += bhi(v[j]); }
      c0 = half_sum(c0); c1 = half_sum(c1);
      if (l < 32) __builtin_amdgcn_raw_buffer_store_b32(__builtin_bit_cast(u32, l < 16 ? c0 : c1), rw, l * 4, O_CB + (id & 15) * 5632 + (id >> 4) * 128, SC1);
    } else if (wv == 7) {
      u32x4 q;
#pragma unroll
      for (int i = 0; i < 4; ++i) q[i] = na[i] | (nb[i] << 16);
      if (l < 2) bst(q, rw, 8192 + l * 16, O_WT + bid * PST);
    }
    SB();
    ST(2);
    float s1 = 0.f, s2 = 0.f;
#pragma unroll
    for (int j = 0; j < 4; ++j) { s1 += d0[j] + d1[j]; s2 = fmaf(d0[j], d0[j], fmaf(d1[j], d1[j], s2)); }
    s1 = wsum(s1); s2 = wsum(s2);
    float mb, inv; lnstat(s1, s2, mb, inv);
    u32x4 a;
#pragma unroll
    for (int j = 0; j < 4; ++j) a[j] = pk((d0[j] - mb) * inv, (d1[j] - mb) * inv);
    f32x4 yr = mm(a, C1, z4), yi = mm(a, S1, z4);
    u32x4 b;
#pragma unroll
    for (int j = 0; j < 4; ++j) b[j] = pk(yr[j] * tc[j] + yi[j] * ts[j], yi[j] * tc[j] - yr[j] * ts[j]);
    f32x4 xr = mm(R2, b, z4), xi = mm(I2, b, z4), sr = mm(SR, b, z4), si = mm(SI, b, z4);
    u32x4 o;
#pragma unroll
    for (int j = 0; j < 4; ++j) o[j] = pk(r == 0 ? sr[j] : xr[j], r == 0 ? si[j] : xi[j]);
    ldsw(sm + L_HT + wv * 1040 + l * 16, o);
    if (t < 352) *(LDS u32*)(sm + L_BB + t * 4) = bv << 16;
    ST(3);
    WAITV(0);
    bar();
    ST(4);
    { u32x4 q; q[3] = tag;
#pragma unroll
      for (int j = 0; j < 3; ++j) q[j] = *(LDS u32*)(sm + L_HT + (3 * i0p + j) * 1040 + i0q * 4);
      bst(q, rw, t * 16, O_X1 + bid * PS1);
      if (wv < 4) {
#pragma unroll
        for (int j = 0; j < 3; ++j) q[j] = *(LDS u32*)(sm + L_HT + (3 * i1p + j) * 1040 + i1q * 4);
        bst(q, rw, t * 16 + 8192, O_X1 + bid * PS1);
      } }
    SB();
#pragma unroll
    for (int i = 0; i < 4; ++i) xf[i] = *(const u32x4*)(x + (srow0 + 32 * wv + i) * D_ + 32 * r + 8 * h);
    if (t2) {
      const u32 o2 = O_W2 + ((id & 7) * 172 + (id >> 3)) * 1024;
      bst(p0, rw, l * 16, o2); bst(p1, rw, l * 16, o2 + 86 * 1024);
    }
#pragma unroll
    for (int c = 0; c < 3; ++c) if (c < 2 || has3) bst(P4, rw, l * 16, O_HB + (g * 88 + ft0 + 8 * c) * 1024);
    SB();
  }

  u32x4 wf[2][16];
  {
    const int w = wv & 3, job = wv >> 2;
    const bool act = job == 0 || cb == 0;
    u32x4 wt[2], R1[4], I1[4], R2[2][2], I2[2][2];
    float tc[2][4], ts[2][4], uc[2][4], us[2][4];
    if (act) {
      const int k1 = 16 * w + r;
      const u32 odd = (r & 1) ? 0x80008000u : 0u, two = (r & 2) ? 0x80008000u : 0u;
#pragma unroll
      for (int i = 0; i < 4; ++i) {
        const int m = 4 * h + i;
        float a = (float)((k1 * m) & 63) * (1.f / 64.f), c = cosr(a), sn = sinr(a);
        u32 R = pk(c, sn), I = pk(-sn, c);
        R1[0][i] = R; I1[0][i] = I; R1[2][i] = R ^ odd; I1[2][i] = I ^ odd;
        u32 Rq = ((r & 1) ? I : R) ^ two, Iq = ((r & 1) ? R : I) ^ two ^ odd;
        R1[1][i] = Rq; I1[1][i] = Iq; R1[3][i] = Rq ^ odd; I1[3][i] = Iq ^ odd;
        a = (float)((r * m) & 31) * (1.f / 32.f); c = cosr(a); sn = sinr(a);
        R = pk(c, sn); I = pk(-sn, c);
        const u32 fi = (i & 1) ? 0x80008000u : 0u;
        R2[0][0][i] = R; I2[0][0][i] = I; R2[0][1][i] = R ^ odd; I2[0][1][i] = I ^ odd;
        R2[1][0][i] = R ^ fi; I2[1][0][i] = I ^ fi; R2[1][1][i] = R ^ odd ^ fi; I2[1][1][i] = I ^ odd ^ fi;
#pragma unroll
        for (int u = 0; u < 2; ++u) {
          a = (float)(k1 * (16 * u + m)) * (1.f / 2048.f); tc[u][i] = cosr(a) * 0x1p-20f; ts[u][i] = sinr(a) * 0x1p-20f;
          a = (float)((16 * w + m) * (16 * u + r)) * (1.f / 2048.f); uc[u][i] = cosr(a); us[u][i] = sinr(a);
        }
      }
    }
    SB();
    ST(5);
    WAITV(0);
    ST(6);
    {
      const int tt = t - 256, jq = ((tt + 512) * 21846) >> 16, jp = tt + 512 - 3 * jq;
      u32x4 g0, g1;
      for (int it = 0; it < SPIN; ++it) {
        asm volatile("" ::: "memory");
        g0 = bld(rw, i0q * PS1 + i0p * 16, O_X1 + cb * 48);
        bool ok;
        if (job) { g1 = bld(rw, jq * PS1 + jp * 16, O_X1 + cb * 48); ok = g0[3] == tag && g1[3] == tag; } else ok = g0[3] == tag;
        if (__all(ok)) break;
        __builtin_amdgcn_s_sleep(2);
      }
      LDS char* p = sm + L_B0 + i0q * 32 + i0p * 12;
      *(LDS u32*)p = g0[0]; *(LDS u32*)(p + 4) = g0[1]; if (i0p != 2) *(LDS u32*)(p + 8) = g0[2];
      if (job) { p = sm + L_B0 + jq * 32 + jp * 12; *(LDS u32*)p = g1[0]; *(LDS u32*)(p + 4) = g1[1]; if (jp != 2) *(LDS u32*)(p + 8) = g1[2]; }
    }
    bar();
    ST(7);
    if (act) {
#pragma unroll
      for (int u = 0; u < 2; ++u) wt[u] = bld(rw, ((2 * u + (h >> 1)) * 64 + 16 * w + r) * PST + (h & 1) * 16, O_WT + (job ? 8192 : cb * 32));
      u32x4 bf[2];
#pragma unroll
      for (int u = 0; u < 2; ++u) {
        f32x4 yr = z4, yi = z4;
#pragma unroll
        for (int q = 0; q < 4; ++q) {
          u32x4 a = ldsr(sm + L_B0 + ((2 * q + (h >> 1)) * 32 + 16 * u + r) * 32 + (h & 1) * 16);
          if (cb == 0) {
#pragma unroll
            for (int i = 0; i < 4; ++i) a[i] = job ? a[i] >> 16 : a[i] & 0xFFFFu;
          }
          yr = mm(a, R1[q], yr); yi = mm(a, I1[q], yi);
        }
#pragma unroll
        for (int j = 0; j < 4; ++j) bf[u][j] = pk(yr[j] * tc[u][j] + yi[j] * ts[u][j], yi[j] * tc[u][j] - yr[j] * ts[u][j]);
      }
      u32x4 ef[2];
#pragma unroll
      for (int u = 0; u < 2; ++u) {
        f32x4 xr = mm(R2[u][1], bf[1], mm(R2[u][0], bf[0], z4)), xi = mm(I2[u][1], bf[1], mm(I2[u][0], bf[0], z4));
#pragma unroll
        for (int j = 0; j < 4; ++j) { float a = blo(wt[u][j]), b = bhi(wt[u][j]); ef[u][j] = pk(xr[j] * a - xi[j] * b, -(xr[j] * b + xi[j] * a)); }
      }
#pragma unroll
      for (int u = 0; u < 2; ++u) {
        f32x4 yr = mm(ef[1], R2[u][1], mm(ef[0], R2[u][0], z4)), yi = mm(ef[1], I2[u][1], mm(ef[0], I2[u][0], z4));
        u32x4 o;
#pragma unroll
        for (int j = 0; j < 4; ++j) o[j] = pk(yr[j] * uc[u][j] + yi[j] * us[u][j], yi[j] * uc[u][j] - yr[j] * us[u][j]);
        ldsw(sm + L_B1 + job * 8192 + ((u * 4 + w) * 64 + l) * 16, o);
      }
    }
    if (job) {
      if (cb == 0) bar();
#pragma unroll
      for (int i = 0; i < NDMA1; ++i) dma(rw, L_H + (w + 4 * i) * 1024, l * 16 + (i & 3) * 4096, O_W1 + (22 * jb + 16 + (i >> 2)) * 16384 + w * 1024);
      if (cb != 0) bar();
#pragma unroll
      for (int i = NDMA1; i < 24; ++i) dma(rw, L_H + (w + 4 * i) * 1024, l * 16 + (i & 3) * 4096, O_W1 + (22 * jb + 16 + (i >> 2)) * 16384 + w * 1024);
      const int tt = t - 256;
      f32x4 cs = z4;
      if (w < 3) {
#pragma unroll
        for (int i = 0; i < 8; ++i) cs += __builtin_bit_cast(f32x4, bld(rw, ((tt & 1) * 8 + i) * 5632 + (tt >> 1) * 16, O_CB + jb * 1408));
#pragma unroll
        for (int j = 0; j < 4; ++j) cs[j] += dpp<0xB1>(cs[j]);
        if (tt < 176) *(LDS f32x4*)(sm + L_CS + (tt >> 1) * 16) = cs;
      }
    } else {
      bar();
      ST(8);
#pragma unroll
      for (int u = 0; u < 2; ++u) {
        f32x4 zr = z4, zi = z4;
        if (cb != 0) {
#pragma unroll
          for (int q = 0; q < 4; ++q) { u32x4 a = ldsr(sm + L_B1 + ((u * 4 + q) * 64 + l) * 16); zr = mm(a, R1[q], zr); zi = mm(a, I1[q], zi); }
          zi = -zi;
        } else {
#pragma unroll
          for (int q = 0; q < 4; ++q) {
            zr = mm(ldsr(sm + L_B1 + ((u * 4 + q) * 64 + l) * 16), R1[q], zr);
            zi = mm(ldsr(sm + L_B1 + 8192 + ((u * 4 + q) * 64 + l) * 16), R1[q], zi);
          }
        }
        u32x4 o;
#pragma unroll
        for (int j = 0; j < 4; ++j) o[j] = min(pk(zr[j], zi[j]), POISON - 1);
        bst(o, rw, ((w * 2 + u) * 64 + l) * 16, O_X2 + cb * PST);
      }
      if (bid == 0 && t == 0) __builtin_amdgcn_raw_buffer_store_b32(tag, rw, 0, O_EP, SC1);
    }
    SB();
#pragma unroll
    for (int ks = 0; ks < NW1A; ++ks) wf[0][ks] = bld(rw, l * 16 + (ks & 3) * 1024, O_W1 + ft0 * 16384 + (ks >> 2) * 4096);
    SB();
    ST(9);
  }

  {
    u32x4 R, I, SA, SB_, M0, M1; float tc[4], ts[4];
#pragma unroll
    for (int i = 0; i < 4; ++i) {
      const int m = 4 * h + i;
      float a = (float)((r * m) & 15) * (1.f / 16.f), c = cosr(a), sn = sinr(a);
      const u32 sg = h >= 2 ? 0x80000000u : 0u;
      R[i] = pk(c, -sn) ^ sg; I[i] = pk(sn, c) ^ sg;
      a = (float)((r * m) & 31) * (1.f / 32.f);
      u32 v = pk(2.f * cosr(a), -2.f * sinr(a));
      if (i == 0) v = h == 0 ? ((r & 1) ? 0xBF803F80u : 0x3F803F80u) : v;
      SA[i] = (i & 1) ? 0u : v; SB_[i] = (i & 1) ? v : 0u;
      a = (float)(r * m) * (1.f / 512.f); tc[i] = cosr(a); ts[i] = sinr(a);
      M0[i] = pk(2.f * c, -2.f * sn);
      a = (float)((m * (2 * r + 1)) & 31) * (1.f / 32.f);
      M1[i] = pk(2.f * cosr(a), -2.f * sinr(a));
      if (i == 0) { M0[i] = h == 0 ? 0x3F803F80u : M0[i]; M1[i] = h == 0 ? 0xBF803F80u : M1[i]; }
    }
    const u32x4 E0 = selfrag(r, h, 0), E1 = selfrag(r, h, 1);
    f32x4 y0[4], y1[4];
#pragma unroll
    for (int i = 0; i < 4; ++i) { y0[i] = mm(E0, xf[i], z4); y1[i] = mm(E1, xf[i], z4); }
    SB();
    WAITV(0);
    bar();
    ST(10);
    u32x4 zin[4];
    for (int it = 0; it < SPIN; ++it) {
      asm volatile("" ::: "memory");
#pragma unroll
      for (int i = 0; i < 4; ++i) zin[i] = bld(rw, (t >> 3) * PST + (t & 7) * 16, O_X2 + i * 64 * PST + g * 128);
      if (__all(clean(zin[0]) && clean(zin[1]) && clean(zin[2]) && clean(zin[3]))) break;
      __builtin_amdgcn_s_sleep(2);
    }
#pragma unroll
    for (int i = 0; i < 4; ++i) ldsw(sm + L_HT + (t & 7) * 4160 + ((t >> 3) & 3) * 1024 + ((16 * i + (t >> 5) + ((t & 7) >> 1)) & 63) * 16, zin[i]);
    bar();
    ST(11);
    u32x4 q[4];
#pragma unroll
    for (int j = 0; j < 4; ++j) q[j] = ldsr(sm + L_HT + wv * 4160 + j * 1024 + ((l + (wv >> 1)) & 63) * 16);
#pragma unroll
    for (int i = 0; i < 4; ++i) {
      SB();
#pragma unroll
      for (int e = NW1A + (32 - NW1A) * i / 4; e < NW1A + (32 - NW1A) * (i + 1) / 4; ++e) {
        const int n = e - NW1A, c = n < NW1A ? 1 : ((n - NW1A) & 1), ks = n < NW1A ? n : NW1A + ((n - NW1A) >> 1);
        wf[c][ks] = bld(rw, l * 16 + (ks & 3) * 1024, O_W1 + (ft0 + 8 * c) * 16384 + (ks >> 2) * 4096);
      }
      SB();
      const u32x4 z = {q[0][i], q[1][i], q[2][i], q[3][i]};
      f32x4 ur = mm(z, R, z4), ui = mm(z, I, z4), sa = mm(z, SA, z4), sb = mm(z, SB_, z4);
      ur[0] = h == 0 ? sa[0] : ur[0]; ui[0] = h == 0 ? sb[0] : ui[0];
      u32x4 v;
#pragma unroll
      for (int j = 0; j < 4; ++j) v[j] = pk(ur[j] * tc[j] - ui[j] * ts[j], ui[j] * tc[j] + ur[j] * ts[j]);
      const f32x4 a = mm(v, M0, y0[i]), b = mm(v, M1, y1[i]);
      ldsw(sm + L_HT + (4 * wv + i) * 1040 + l * 16, u32x4{pk(a[0], a[1]), pk(a[2], a[3]), pk(b[0], b[1]), pk(b[2], b[3])});
    }
    bar();
    ST(12);
  }

  const int cg = wv & 3, kh = wv >> 2;
  u32x4 w2f[43];
  const u32 w2b = O_W2 + ((4 * jb + cg) * 86 + kh) * 1024;
#define W2LD(i) do { int n_ = (i) + 11 * jb; n_ = n_ >= 43 ? n_ - 43 : n_; w2f[i] = bld(rw, l * 16, w2b + n_ * 2048); } while (0)
  {
    f32x4 acc[3][2];
#pragma unroll
    for (int c = 0; c < 3; ++c) { acc[c][0] = z4; acc[c][1] = z4; }
    const u32x4 ones = {0x3F803F80u, 0x3F803F80u, 0x3F803F80u, 0x3F803F80u};
    const bool gm = wv >= 6;
    LDS char* third = gm ? sm + L_HT + ((wv - 6) * 16 + r) * 1040 + h * 256 : sm + L_H + wv * 16384 + l * 16;
    const int tst = gm ? 16 : 1024;
#pragma unroll
    for (int ks = 0; ks < 16; ++ks) {
      const u32x4 a0 = ldsr(sm + L_HT + r * 1040 + (h * 16 + ks) * 16), a1 = ldsr(sm + L_HT + (16 + r) * 1040 + (h * 16 + ks) * 16);
      const u32x4 w3 = ldsr(third + ks * tst);
#pragma unroll
      for (int c = 0; c < 2; ++c) { acc[c][0] = mm(wf[c][ks], a0, acc[c][0]); acc[c][1] = mm(wf[c][ks], a1, acc[c][1]); }
      if (gm) { acc[2][0] = mm(w3, w3, acc[2][0]); acc[2][1] = mm(ones, w3, acc[2][1]); }
      else { acc[2][0] = mm(w3, a0, acc[2][0]); acc[2][1] = mm(w3, a1, acc[2][1]); }
      if (ks >= 16 - NW2A) W2LD(ks - (16 - NW2A));
    }
    if (gm) {
      const int e = r & 3;
      const float s2 = e == 0 ? acc[2][0][0] : e == 1 ? acc[2][0][1] : e == 2 ? acc[2][0][2] : acc[2][0][3];
      float mb, inv; lnstat(acc[2][1][0], s2, mb, inv);
      if (h == (r >> 2)) *(LDS f32x2*)(sm + L_ST + ((wv - 6) * 16 + r) * 8) = f32x2{mb, inv};
    }
    bar();
    ST(13);
    const f32x2 st0 = *(LDS f32x2*)(sm + L_ST + r * 8), st1 = *(LDS f32x2*)(sm + L_ST + (16 + r) * 8);
#pragma unroll
    for (int c = 0; c < 3; ++c) {
      if (c < 2 || has3) {
        const int ft = ft0 + 8 * c;
        const f32x4 cs = *(LDS f32x4*)(sm + L_CS + (wv + 8 * c) * 64 + h * 16), bb = *(LDS f32x4*)(sm + L_BB + (wv + 8 * c) * 64 + h * 16);
        float v0[4], v1[4];
#pragma unroll
        for (int j = 0; j < 4; ++j) {
          float p0 = fmaxf((acc[c][0][j] - st0[0] * cs[j]) * st0[1] + bb[j], 0.f), p1 = fmaxf((acc[c][1][j] - st1[0] * cs[j]) * st1[1] + bb[j], 0.f);
          v0[j] = p0 * p0; v1[j] = p1 * p1;
        }
        auto s0 = __builtin_amdgcn_permlane16_swap(pk(v0[0], v0[1]), pk(v1[0], v1[1]), false, false);
        auto s1 = __builtin_amdgcn_permlane16_swap(pk(v0[2], v0[3]), pk(v1[2], v1[3]), false, false);
        const u32x4 o = {(u32)s0[0] & 0x7FFFFFFFu, (u32)s1[0] & 0x7FFFFFFFu, (u32)s0[1] & 0x7FFFFFFFu, (u32)s1[1] & 0x7FFFFFFFu};
        bst(o, rw, l * 16, O_HB + (g * 88 + ft) * 1024);
        ldsw(sm + L_H + ft * 1024 + l * 16, o);
      }
    }
  }
  SB();
#pragma unroll
  for (int i = NW2A; i < NW2B; ++i) W2LD(i);
  const u32x4 bq = *(const u32x4*)(b2 + 128 * jb + 8 * (t & 15));
  SB();
  bar();
  ST(14);

  {
    f32x16 acc = {0.f};
    LDS char* hb = sm + L_H + kh * 1024 + l * 16;
#pragma unroll
    for (int i = 0; i < 10; ++i) acc = mm32(w2f[i], ldsr(hb + (i + 11 * jb) * 2048), acc);
    SB();
    asm volatile("s_waitcnt vmcnt(%0)" :: "n"(NW2B - NW2A + 1) : "memory");
    ST(15);
    u32x4 hf[9];
    const int f0 = (jb == 3 ? 0 : 22 * jb + 22) + wv;
    const bool nine = jb == 3 && wv < 2;
    for (int it = 0; it < SPIN; ++it) {
      asm volatile("" ::: "memory");
      bool ok = true;
#pragma unroll
      for (int i = 0; i < 8; ++i) { int f = f0 + 8 * i; f = f >= 86 ? f - 86 : f; hf[i] = bld(rw, l * 16, O_HB + (g * 88 + f) * 1024); }
      if (nine) { hf[8] = bld(rw, l * 16, O_HB + (g * 88 + 64 + wv) * 1024); ok = clean(hf[8]); }
#pragma unroll
      for (int i = 0; i < 8; ++i) ok = ok && clean(hf[i]);
      if (__all(ok)) break;
      __builtin_amdgcn_s_sleep(2);
    }
    SB();
#pragma unroll
    for (int i = 0; i < 8; ++i) { int f = f0 + 8 * i; f = f >= 86 ? f - 86 : f; ldsw(sm + L_H + f * 1024 + l * 16, hf[i]); if (NW2B + i < 43) W2LD(NW2B + i); }
    if (nine) ldsw(sm + L_H + (64 + wv) * 1024 + l * 16, hf[8]);
    bar();
    ST(16);
#pragma unroll
    for (int i = 10; i < 43; ++i) {
      int n = i + 11 * jb; n = n >= 43 ? n - 43 : n;
      acc = mm32(w2f[i], ldsr(hb + n * 2048), acc);
      if (NW2B + 8 + (i - 10) < 43) W2LD(NW2B + 8 + (i - 10));
    }
#pragma unroll
    for (int q = 0; q < 4; ++q)
      *(LDS f32x4*)(sm + L_RED + (kh * 32 + (l & 31)) * 528 + (32 * cg + 8 * q + 4 * (l >> 5)) * 4) = f32x4{acc[4 * q], acc[4 * q + 1], acc[4 * q + 2], acc[4 * q + 3]};
    bar();
    ST(17);
    const int tok = t >> 4, e = t & 15, d0 = 128 * jb + 8 * e;
    LDS char* rp = sm + L_RED + tok * 528 + (e >> 3) * 256 + (e & 7) * 16;
    const f32x4 a0 = *(LDS f32x4*)rp + *(LDS f32x4*)(rp + 32 * 528), a1 = *(LDS f32x4*)(rp + 128) + *(LDS f32x4*)(rp + 32 * 528 + 128);
    LDS char* hp = sm + L_HT + tok * 1040 + (((d0 >> 2) & 3) * 16 + (d0 >> 5)) * 16 + ((d0 >> 4) & 1) * 8;
    const u32x2 h0 = *(LDS u32x2*)hp, h1 = *(LDS u32x2*)(hp + 256);
    u32x4 o;
    o[0] = pk(a0[0] + blo(bq[0]) + blo(h0[0]), a1[0] + bhi(bq[0]) + bhi(h0[0]));
    o[1] = pk(a0[1] + blo(bq[1]) + blo(h0[1]), a1[1] + bhi(bq[1]) + bhi(h0[1]));
    o[2] = pk(a0[2] + blo(bq[2]) + blo(h1[0]), a1[2] + bhi(bq[2]) + bhi(h1[0]));
    o[3] = pk(a0[3] + blo(bq[3]) + blo(h1[1]), a1[3] + bhi(bq[3]) + bhi(h1[1]));
    *(u32x4*)(out + (srow0 + 32 * (tok >> 2) + (tok & 3)) * D_ + d0) = o;
  }
}

extern "C" void kernel_launch(void* const* d_in, const int* in_sizes, int n_in, void* d_out, int out_size, void* d_ws, size_t ws_size, hipStream_t stream) {
  (void)hipFuncSetAttribute((const void*)k_all, hipFuncAttributeMaxDynamicSharedMemorySize, L_END);
  k_all<<<256, 512, L_END, stream>>>((const uint16_t*)d_in[0], (const uint16_t*)d_in[1], (const uint16_t*)d_in[2], (const uint16_t*)d_in[4], (const uint16_t*)d_in[5],
                                     (const uint16_t*)d_in[6], (const uint16_t*)d_in[7], (char*)d_ws, (uint16_t*)d_out);
}
```

```cpp
#define HPOIS 0
#define X1BAR 0
#define XFE 1
#define NJ1 16
#define NJ2 24
#define NW1A 8
#define NW2A 16
#define NW2B 22
#define NW2C 26
#define TCH1 0
#define TCH2 0
#define NDM 24
#include <hip/hip_runtime.h>
#include <hip/hip_bf16.h>
#include <stdint.h>

#ifdef __HIP_DEVICE_COMPILE__
#pragma clang attribute push(__attribute__((target("no-packed-fp32-ops"))), apply_to = function)
#endif
typedef unsigned u32;
typedef u32 u32x2 __attribute__((ext_vector_type(2)));
typedef u32 u32x4 __attribute__((ext_vector_type(4)));
typedef float f32x4 __attribute__((ext_vector_type(4)));
typedef float f32x2 __attribute__((ext_vector_type(2)));
typedef float f32x16 __attribute__((ext_vector_type(16)));
typedef __bf16 bf16x8 __attribute__((ext_vector_type(8)));
typedef __bf16 bf16x2 __attribute__((ext_vector_type(2)));
typedef __amdgpu_buffer_rsrc_t rsrc_t;
#define LDS __attribute__((address_space(3)))
#define GLB __attribute__((address_space(1)))
#define DEV __device__ __forceinline__

constexpr int S_ = 2048, D_ = 512, F_ = 1376, DF_ = 257;
constexpr u32 PST = 8448, PS1 = 12544;
constexpr u32 O_X1 = 0, O_WT = 0x320000, O_X2 = 0x540000, O_W1 = 0x760000, O_W2 = 0x8C0000,
              O_CB = 0xA20000, O_HB = 0xA40000, O_EP = 0x1000000, WS_END = 0x1001000;
constexpr u32 POISON = 0xFFFFFFFFu;
constexpr int SC1 = 16;
constexpr int SPIN = 1 << 15;
#define ST(i)
#define PIN(x) asm volatile("" : "+v"(x))
#define SB() __builtin_amdgcn_sched_barrier(0)
#define WAITV(n) asm volatile("s_waitcnt vmcnt(" #n ")" ::: "memory")

DEV rsrc_t mkrs(const void* p, u32 bytes) { return __builtin_amdgcn_make_buffer_rsrc((void*)p, 0, bytes, 0x00020000); }
DEV u32 pk(float a, float b) { f32x2 v = {a, b}; return __builtin_bit_cast(u32, __builtin_convertvector(v, bf16x2)); }
DEV float blo(u32 v) { return __builtin_bit_cast(float, v << 16); }
DEV float bhi(u32 v) { return __builtin_bit_cast(float, v & 0xFFFF0000u); }
DEV float rbf(float a) { return blo(pk(a, a)); }
DEV float cosr(float rev) { return __builtin_amdgcn_cosf(rev); }
DEV float sinr(float rev) { return __builtin_amdgcn_sinf(rev); }
DEV f32x4 mm(u32x4 a, u32x4 b, f32x4 c) { return __builtin_amdgcn_mfma_f32_16x16x32_bf16(__builtin_bit_cast(bf16x8, a), __builtin_bit_cast(bf16x8, b), c, 0, 0, 0); }
DEV f32x16 mm32(u32x4 a, u32x4 b, f32x16 c) { return __builtin_amdgcn_mfma_f32_32x32x16_bf16(__builtin_bit_cast(bf16x8, a), __builtin_bit_cast(bf16x8, b), c, 0, 0, 0); }
template <int C> DEV float dpp(float x) { return __builtin_bit_cast(float, __builtin_amdgcn_mov_dpp(__builtin_bit_cast(int, x), C, 0xf, 0xf, true)); }
DEV float wsum(float v) {
  v += dpp<0xB1>(v); v += dpp<0x4E>(v); v += dpp<0x141>(v); v += dpp<0x128>(v);
  u32 u = __builtin_bit_cast(u32, v);
  auto a = __builtin_amdgcn_permlane16_swap(u, u, false, false);
  v = __builtin_bit_cast(float, (u32)a[0]) + __builtin_bit_cast(float, (u32)a[1]);
  u = __builtin_bit_cast(u32, v);
  auto b = __builtin_amdgcn_permlane32_swap(u, u, false, false);
  return __builtin_bit_cast(float, (u32)b[0]) + __builtin_bit_cast(float, (u32)b[1]);
}
DEV u32x4 ldsr(LDS char* p) { return *(LDS u32x4*)p; }
DEV void ldsw(LDS char* p, u32x4 v) { *(LDS u32x4*)p = v; }
DEV u32 umax(u32 a, u32 b) { return a > b ? a : b; }
DEV u32 umin(u32 a, u32 b) { return a < b ? a : b; }
DEV bool all(bool p) { return __builtin_amdgcn_ballot_w64(!p) == 0; }
DEV void bar() { asm volatile("s_waitcnt lgkmcnt(0)" ::: "memory"); __builtin_amdgcn_s_barrier(); asm volatile("" ::: "memory"); }
DEV u32x4 selfrag(int r, int h, int p) {
  u32x4 s;
#pragma unroll
  for (int i = 0; i < 4; ++i) { int e = 16 * p + r - 8 * h - 2 * i; s[i] = e == 0 ? 0x3F80u : (e == 1 ? 0x3F800000u : 0u); }
  return s;
}
DEV void lnstat(float s1, float s2, float& mb, float& inv) {
  float m = s1 * (1.f / 512.f);
  float v = __builtin_fmaxf(s2 * (1.f / 512.f) - m * m, 0.f);
  mb = rbf(m);
  inv = __builtin_amdgcn_rcpf(rbf(__builtin_amdgcn_sqrtf(rbf(rbf(v) + 1e-5f))));
}
DEV int kd_of(int cb) { int l = cb >> 2, k1 = l & 15, k2 = 4 * (l >> 4) + (cb & 3); return k1 == 0 ? 16 * k2 : (k2 < 8 ? k1 + 32 * k2 : 512 - k1 - 32 * k2); }

DEV float half_sum(float c) {
  u32 u = __builtin_bit_cast(u32, c);
  auto a = __builtin_amdgcn_permlane16_swap(u, u, false, false);
  c = __builtin_bit_cast(float, (u32)a[0]) + __builtin_bit_cast(float, (u32)a[1]);
  u = __builtin_bit_cast(u32, c);
  auto b = __builtin_amdgcn_permlane32_swap(u, u, false, false);
  return __builtin_bit_cast(float, (u32)b[0]) + __builtin_bit_cast(float, (u32)b[1]);
}
DEV u32x4 bld(rsrc_t w, u32 v, u32 s) { return __builtin_amdgcn_raw_buffer_load_b128(w, v, s, SC1); }
DEV void bst(u32x4 d, rsrc_t w, u32 v, u32 s) { __builtin_amdgcn_raw_buffer_store_b128(d, w, v, s, SC1); asm volatile("s_nop 1" :: "v"(d) : "memory"); }
DEV void dma(rsrc_t w, u32 lds, u32 v, u32 s) { asm volatile("s_mov_b32 m0, %0\n\ts_nop 0\n\tbuffer_load_dwordx4 %1, %2, %3 offen sc1 lds" :: "s"(lds), "v"(v), "s"(w), "s"(s) : "memory"); }
DEV void dmap(rsrc_t w, u32 lds, u32 v, u32 s) { asm volatile("s_mov_b32 m0, %0\n\ts_nop 0\n\tbuffer_load_dwordx4 %1, %2, %3 offen lds" :: "s"(lds), "v"(v), "s"(w), "s"(s) : "memory"); }
DEV void touch(rsrc_t w, u32 lds, u32 v, u32 s) { asm volatile("s_mov_b32 m0, %0\n\ts_nop 0\n\tbuffer_load_dword %1, %2, %3 offen sc1 lds" :: "s"(lds), "v"(v), "s"(w), "s"(s) : "memory"); }
typedef short s16x4 __attribute__((ext_vector_type(4)));
DEV u32x2 ldst(LDS char* p) { return __builtin_bit_cast(u32x2, __builtin_amdgcn_ds_read_tr16_b64_v4i16((LDS s16x4*)p)); }
typedef u32x2 u32x2u __attribute__((aligned(2)));
template <int N> struct IC { static constexpr int v = N; };
DEV bool clean(u32x4 a) { return umax(umax(a[0], a[1]), umax(a[2], a[3])) != POISON; }

constexpr int L_H = 0;
constexpr int L_RED = 90112;
constexpr int L_B0 = 98304, L_B1 = L_B0 + 8192;
constexpr int L_HT = 123904;
constexpr int L_ST = L_HT + 33280;
constexpr int L_CS = L_ST + 256, L_BB = L_CS + 1408;
constexpr int L_DUM = L_BB + 1408, L_END = L_DUM + 256;

__global__ __launch_bounds__(512, 2) void k_all(const uint16_t* __restrict__ x, const uint16_t* __restrict__ wr, const uint16_t* __restrict__ wi,
                                                const uint16_t* __restrict__ w1, const uint16_t* __restrict__ b1, const uint16_t* __restrict__ w2,
                                                const uint16_t* __restrict__ b2, char* ws, uint16_t* __restrict__ out) {
  extern __shared__ __attribute__((aligned(16))) char smem_[];
  LDS char* sm = (LDS char*)smem_;
  ST(0);
  asm volatile("" : "+s"(x), "+s"(wr), "+s"(wi), "+s"(w1), "+s"(b1), "+s"(w2), "+s"(b2), "+s"(ws), "+s"(out));
  const int t = threadIdx.x, l = t & 63, wv = __builtin_amdgcn_readfirstlane(t >> 6), r = l & 15, h = l >> 4;
  const int bid = blockIdx.x;
  const int cb = (bid & 7) * 32 + (bid >> 3), g = cb >> 2, jb = cb & 3;
  const rsrc_t rw = mkrs(ws, WS_END);
  const f32x4 z4 = {0.f, 0.f, 0.f, 0.f};
  const u32x4 P4 = {POISON, POISON, POISON, POISON};
  const int srow0 = 512 * (g >> 4) + 256 * (g & 1) + 16 * ((g >> 3) & 1) + 4 * ((g >> 1) & 3);
  const int ft0 = 22 * jb + wv;
  const bool has3 = wv < 6 && ft0 + 16 < 86;
  const u32 tag = *(const __attribute__((address_space(4))) u32*)(ws + O_EP) + 1u;
  const int i0q = (t * 21846) >> 16, i0p = t - 3 * i0q, i1q = ((t + 512) * 21846) >> 16, i1p = t + 512 - 3 * i1q;
  u32x4 xf[4];

  {
    const rsrc_t rx = mkrs(x, S_ * D_ * 2);
    const u32x4 xa = __builtin_amdgcn_raw_buffer_load_b128(rx, 64 * r + 16 * h, (256 * (bid >> 5) + 32 * wv + (bid & 31)) * (D_ * 2), 0);
    const int ksr = ((bid & 63) + 64 * (8 * (bid >> 6) + wv)) * DF_;
    const u32x2 wa = *(const GLB u32x2u*)((const GLB uint16_t*)wr + ksr + 4 * l), wb = *(const GLB u32x2u*)((const GLB uint16_t*)wi + ksr + 4 * l);
    const u32 na = ((const GLB uint16_t*)wr)[ksr + 256], nb = ((const GLB uint16_t*)wi)[ksr + 256];
    SB();
    u32 v[8];
    const int id = (wv < 3 ? wv : wv - 3) * 256 + bid, tp = id >> 4;
    const bool t1 = wv < 3 && id < 688, t2 = wv >= 3 && id < 688, nat = tp - 11 * ((tp * 47) >> 9) >= 8;
    {
      const rsrc_t rt = mkrs(wv < 3 ? w1 : w2, id < 688 ? D_ * F_ * 2 : 0);
      const int kp = (id >> 3) & 1, st = wv < 3 ? F_ * 2 : (nat ? D_ * 2 : D_ * 4), s4 = wv < 3 ? 16 * st : 4 * st;
      const u32 so = wv < 3 ? (id & 15) * (64 * F_) + 64 * tp : (32 * tp + (nat ? 16 * kp : kp)) * (D_ * 2) + 128 * (id & 7);
      const u32 vo = wv < 3 ? h * (8 * F_) + 4 * r : (l >> 5) * 8 * st + 4 * (l & 31);
#pragma unroll
      for (int j = 0; j < 8; ++j) v[j] = __builtin_amdgcn_raw_buffer_load_b32(rt, vo, so + (j >> 2) * s4 + (j & 3) * st, 0);
    }
    const u32 bv = ((const GLB uint16_t*)b1)[umin(352 * jb + (t >= 256 ? t : (t & ~31) + 2 * r + (h & 1)), F_ - 1)];
#pragma unroll
    for (int i = 0; i < 4; ++i) xf[i] = __builtin_amdgcn_raw_buffer_load_b128(rx, 64 * r + 16 * h + i * 1024, (srow0 + 32 * wv) * (D_ * 2), 0);
    bst(P4, rw, t * 16, O_X2 + cb * PST);
    SB();
    ST(19);
    u32x4 C1, S1, R2, I2, SR, SI; float tc[4], ts[4];
#pragma unroll
    for (int i = 0; i < 4; ++i) {
      float a0 = (float)(((8 * h + 2 * i) * r) & 31) * (1.f / 32.f), a1 = (float)(((8 * h + 2 * i + 1) * r) & 31) * (1.f / 32.f);
      C1[i] = pk(cosr(a0), cosr(a1));
      S1[i] = r == 0 ? 0xBF803F80u : pk(-sinr(a0), -sinr(a1));
      const int n2 = 4 * h + i;
      float at = (float)(r * n2) * (1.f / 512.f); tc[i] = cosr(at); ts[i] = sinr(at);
      float ph = (float)((r * n2) & 15) * (1.f / 16.f), c = cosr(ph), sn = sinr(ph);
      R2[i] = pk(c, sn); I2[i] = pk(-sn, c) ^ (r >= 8 ? 0x80008000u : 0u);
      float ps = (float)((r * n2) & 31) * (1.f / 32.f);
      u32 cs = pk(cosr(ps), r == 0 ? ((i & 1) ? -1.f : 1.f) : -sinr(ps));
      SR[i] = (r & 1) ? cs << 16 : cs & 0xFFFFu;
      SI[i] = (r & 1) ? cs & 0xFFFF0000u : cs >> 16;
    }
    u32x4 E0 = selfrag(r, h, 0), E1 = selfrag(r, h, 1);
    int kd = kd_of(t >> 1);
    PIN(C1); PIN(S1); PIN(R2); PIN(I2); PIN(SR); PIN(SI); PIN(E0); PIN(E1); PIN(kd);
#pragma unroll
    for (int i = 0; i < 4; ++i) { PIN(tc[i]); PIN(ts[i]); }
    SB();
    ST(1);
    f32x4 d0 = mm(xa, E0, z4), d1 = mm(xa, E1, z4);
    ldsw(sm + L_B1 + wv * 1040 + l * 16, u32x4{__builtin_amdgcn_perm(wb[0], wa[0], 0x05040100u), __builtin_amdgcn_perm(wb[0], wa[0], 0x07060302u),
                                               __builtin_amdgcn_perm(wb[1], wa[1], 0x05040100u), __builtin_amdgcn_perm(wb[1], wa[1], 0x07060302u)});
    *(LDS u32*)(sm + L_B1 + wv * 1040 + 1024) = na | (nb << 16);
    bar();
    { u32x4 q;
#pragma unroll
      for (int i = 0; i < 4; ++i) q[i] = *(LDS u32*)(sm + L_B1 + (4 * (t & 1) + i) * 1040 + kd * 4);
      bst(q, rw, t * 16, O_WT + bid * PST); }
    u32x4 p0, p1;
#pragma unroll
    for (int i = 0; i < 4; ++i) { p0[i] = __builtin_amdgcn_perm(v[2 * i + 1], v[2 * i], 0x05040100u); p1[i] = __builtin_amdgcn_perm(v[2 * i + 1], v[2 * i], 0x07060302u); }
    if (t1) {
      const u32 o = O_W1 + ((id >> 4) * 32 + (id & 15)) * 1024;
      if (!nat) { bst(p0, rw, l * 16, o); bst(p1, rw, l * 16, o + 16384); }
      const u32x4 ones = {0x3F803F80u, 0x3F803F80u, 0x3F803F80u, 0x3F803F80u};
      const float c0 = mm(ones, p0, z4)[0], c1 = mm(ones, p1, z4)[0];
      if (l < 32) __builtin_amdgcn_raw_buffer_store_b32(__builtin_bit_cast(u32, l < 16 ? c0 : c1), rw, nat ? 8 * r + 4 * h : l * 4, O_CB + (id & 15) * 5632 + (id >> 4) * 128, SC1);
    } else if (wv == 7) {
      u32x4 q;
#pragma unroll
      for (int i = 0; i < 4; ++i) q[i] = *(LDS u32*)(sm + L_B1 + (4 * (t & 1) + i) * 1040 + 1024);
      if (l < 2) bst(q, rw, 8192 + l * 16, O_WT + bid * PST);
    }
    SB();
    ST(2);
    float s1 = 0.f, s2 = 0.f;
#pragma unroll
    for (int j = 0; j < 4; ++j) { s1 += d0[j] + d1[j]; s2 = __builtin_fmaf(d0[j], d0[j], __builtin_fmaf(d1[j], d1[j], s2)); }
    s1 = wsum(s1); s2 = wsum(s2);
    float mb, inv; lnstat(s1, s2, mb, inv);
    u32x4 a;
#pragma unroll
    for (int j = 0; j < 4; ++j) a[j] = pk((d0[j] - mb) * inv, (d1[j] - mb) * inv);
    f32x4 yr = mm(a, C1, z4), yi = mm(a, S1, z4);
    u32x4 b;
#pragma unroll
    for (int j = 0; j < 4; ++j) b[j] = pk(yr[j] * tc[j] + yi[j] * ts[j], yi[j] * tc[j] - yr[j] * ts[j]);
    f32x4 xr = mm(R2, b, z4), xi = mm(I2, b, z4), sr = mm(SR, b, z4), si = mm(SI, b, z4);
    u32x4 o;
#pragma unroll
    for (int j = 0; j < 4; ++j) o[j] = pk(r == 0 ? sr[j] : xr[j], r == 0 ? si[j] : xi[j]);
    ldsw(sm + L_HT + wv * 1040 + l * 16, o);
    if (t < 352) *(LDS u32*)(sm + L_BB + t * 4) = bv << 16;
    ST(3);
    WAITV(0);
    bar();
    ST(4);
    { u32x4 q; q[3] = tag;
#pragma unroll
      for (int j = 0; j < 3; ++j) q[j] = *(LDS u32*)(sm + L_HT + (3 * i0p + j) * 1040 + i0q * 4);
      bst(q, rw, t * 16, O_X1 + bid * PS1);
      if (wv < 4) {
#pragma unroll
        for (int j = 0; j < 3; ++j) q[j] = *(LDS u32*)(sm + L_HT + (3 * i1p + j) * 1040 + i1q * 4);
        bst(q, rw, t * 16 + 8192, O_X1 + bid * PS1);
      } }
    SB();
    if (t2) {
      int n = tp - 11 * ((id & 7) >> 1); n = n < 0 ? n + 43 : n;
      const u32 o2 = O_W2 + ((id & 7) * 176 + ((id >> 3) & 1) * 44 + n) * 1024;
      bst(p0, rw, l * 16, o2); bst(p1, rw, l * 16, o2 + 88 * 1024);
    }
#pragma unroll
    for (int c = 0; c < 3; ++c) if (c < 2 || has3) bst(P4, rw, l * 16, O_HB + (g * 88 + ft0 + 8 * c) * 1024);
    if (wv >= 4) {
      const rsrc_t r1 = mkrs(w1, D_ * F_ * 2);
      u32 vo[3];
#pragma unroll
      for (int m = 0; m < 3; ++m) { const int c = 64 * m + l, k = (c * 5462) >> 16; vo[m] = k * (F_ * 2) + (umin((c - 12 * k) ^ ((k >> 1) & 2), jb == 3 ? 7 : 11) << 4); }
#pragma unroll
      for (int i = 0; i < NDM; ++i) dmap(r1, L_H + ((wv - 4 + 4 * (i / 3)) * 3 + i % 3) * 1024, vo[i % 3], (wv - 4 + 4 * (i / 3)) * (16 * F_ * 2) + 704 * jb + 512);
    }
    SB();
  }

  u32x4 wf[2][16];
  const u32 w1b = O_W1 + ft0 * 16384;
#define W1LD(e) wf[(e) & 1][(e) >> 1] = bld(rw, l * 16 + (((e) >> 1) & 3) * 1024, w1b + ((e) & 1) * 131072 + ((e) >> 3) * 4096)
  const int job = wv >> 2;
  {
    const int w = wv & 3;
    const bool act = job == 0 || cb == 0;
    u32x4 wt[2], R1[4], I1[4], R2[2][2], I2[2][2];
    float tc[2][4], ts[2][4], uc[2][4], us[2][4];
    if (act) {
      int lo = l; PIN(lo);
      const int r = lo & 15, h = lo >> 4;
      const int k1 = 16 * w + r;
      const u32 odd = (r & 1) ? 0x80008000u : 0u, two = (r & 2) ? 0x80008000u : 0u;
#pragma unroll
      for (int i = 0; i < 4; ++i) {
        const int m = 4 * h + i;
        float a = (float)((k1 * m) & 63) * (1.f / 64.f), c = cosr(a), sn = sinr(a);
        u32 R = pk(c, sn), I = pk(-sn, c);
        R1[0][i] = R; I1[0][i] = I; R1[2][i] = R ^ odd; I1[2][i] = I ^ odd;
        u32 Rq = ((r & 1) ? I : R) ^ two, Iq = ((r & 1) ? R : I) ^ two ^ odd;
        R1[1][i] = Rq; I1[1][i] = Iq; R1[3][i] = Rq ^ odd; I1[3][i] = Iq ^ odd;
        a = (float)((r * m) & 31) * (1.f / 32.f); c = cosr(a); sn = sinr(a);
        R = pk(c, sn); I = pk(-sn, c);
        const u32 fi = (i & 1) ? 0x80008000u : 0u;
        R2[0][0][i] = R; I2[0][0][i] = I; R2[0][1][i] = R ^ odd; I2[0][1][i] = I ^ odd;
        R2[1][0][i] = R ^ fi; I2[1][0][i] = I ^ fi; R2[1][1][i] = R ^ odd ^ fi; I2[1][1][i] = I ^ odd ^ fi;
#pragma unroll
        for (int u = 0; u < 2; ++u) {
          a = (float)(k1 * (16 * u + m)) * (1.f / 2048.f); tc[u][i] = cosr(a) * 0x1p-20f; ts[u][i] = sinr(a) * 0x1p-20f;
          a = (float)((16 * w + m) * (16 * u + r)) * (1.f / 2048.f); uc[u][i] = cosr(a); us[u][i] = sinr(a);
          PIN(tc[u][i]); PIN(ts[u][i]); PIN(uc[u][i]); PIN(us[u][i]);
        }
      }
#pragma unroll
      for (int i = 0; i < 4; ++i) { PIN(R1[i]); PIN(I1[i]); PIN(R2[i >> 1][i & 1]); PIN(I2[i >> 1][i & 1]); }
    }
    SB();
    ST(5);
    WAITV(0);
    ST(6);
    {
      const int tt = t - 256, jq = ((tt + 512) * 21846) >> 16, jp = tt + 512 - 3 * jq;
      u32x4 g0, g1; u32 v0 = i0q * PS1 + i0p * 16, v1 = jq * PS1 + jp * 16;
      #pragma unroll 1
    for (int it = 0; it < SPIN; ++it) {
        asm volatile("" : "+v"(v0), "+v"(v1) :: "memory");
        g0 = bld(rw, v0, O_X1 + cb * 48);
        bool ok;
        if (job) { g1 = bld(rw, v1, O_X1 + cb * 48); ok = (g0[3] == tag) & (g1[3] == tag); } else ok = g0[3] == tag;
        if (all(ok)) break;
        __builtin_amdgcn_s_sleep(2);
      }
      LDS char* p = sm + L_B0 + i0q * 32 + i0p * 12;
      *(LDS u32*)p = g0[0]; *(LDS u32*)(p + 4) = g0[1]; if (i0p != 2) *(LDS u32*)(p + 8) = g0[2];
      if (job) { p = sm + L_B0 + jq * 32 + jp * 12; *(LDS u32*)p = g1[0]; *(LDS u32*)(p + 4) = g1[1]; if (jp != 2) *(LDS u32*)(p + 8) = g1[2]; }
    }
    if (cb == 0) {
      bar();
      const u32x4 a = ldsr(sm + L_B0 + t * 16);
      ldsw(sm + L_B0 + t * 16, a & 0xFFFFu); ldsw(sm + L_HT + t * 16, a >> 16);
    }
    bar();
    ST(7);
    if (act) {
#pragma unroll
      for (int u = 0; u < 2; ++u) wt[u] = bld(rw, ((2 * u + (h >> 1)) * 64 + 16 * w + r) * PST + (h & 1) * 16, O_WT + (job ? 8192 : cb * 32));
      u32x4 bf[2], af[2][4];
      LDS char* ap = sm + (job ? L_HT : L_B0) + ((h >> 1) * 32 + r) * 32 + (h & 1) * 16;
#pragma unroll
      for (int u = 0; u < 2; ++u)
#pragma unroll
        for (int q = 0; q < 4; ++q) af[u][q] = ldsr(ap + q * 2048 + u * 512);
      SB();
#pragma unroll
      for (int u = 0; u < 2; ++u) {
        f32x4 yr = z4, yi = z4;
#pragma unroll
        for (int q = 0; q < 4; ++q) { yr = mm(af[u][q], R1[q], yr); yi = mm(af[u][q], I1[q], yi); }
#pragma unroll
        for (int j = 0; j < 4; ++j) bf[u][j] = pk(yr[j] * tc[u][j] + yi[j] * ts[u][j], yi[j] * tc[u][j] - yr[j] * ts[u][j]);
      }
      u32x4 ef[2];
#pragma unroll
      for (int u = 0; u < 2; ++u) {
        f32x4 xr = mm(R2[u][1], bf[1], mm(R2[u][0], bf[0], z4)), xi = mm(I2[u][1], bf[1], mm(I2[u][0], bf[0], z4));
#pragma unroll
        for (int j = 0; j < 4; ++j) { float a = blo(wt[u][j]), b = bhi(wt[u][j]); ef[u][j] = pk(xr[j] * a - xi[j] * b, -(xr[j] * b + xi[j] * a)); }
      }
#pragma unroll
      for (int u = 0; u < 2; ++u) {
        f32x4 yr = mm(ef[1], R2[u][1], mm(ef[0], R2[u][0], z4)), yi = mm(ef[1], I2[u][1], mm(ef[0], I2[u][0], z4));
        u32x4 o;
#pragma unroll
        for (int j = 0; j < 4; ++j) o[j] = pk(yr[j] * uc[u][j] + yi[j] * us[u][j], yi[j] * uc[u][j] - yr[j] * us[u][j]);
        ldsw(sm + L_B1 + job * 8192 + ((u * 4 + w) * 64 + l) * 16, o);
      }
    }
    if (job) {
      if (cb == 0) bar(); else __builtin_amdgcn_s_sleep(1);
      const int tt = t - 256;
      u32x4 cp[8];
      if (w < 3) {
#pragma unroll
        for (int i = 0; i < 8; ++i) cp[i] = bld(rw, ((tt & 1) * 8 + i) * 5632 + (tt >> 1) * 16, O_CB + jb * 1408);
      }
      SB();
#pragma unroll
      for (int e = 0; e < NJ1; ++e) W1LD(e);
      SB();
      if (cb != 0) bar();
#pragma unroll
      for (int e = NJ1; e < NJ2; ++e) W1LD(e);
      SB();
      if (w < 3) {
        f32x4 cs = z4;
#pragma unroll
        for (int i = 0; i < 8; ++i) cs += __builtin_bit_cast(f32x4, cp[i]);
#pragma unroll
        for (int j = 0; j < 4; ++j) cs[j] += dpp<0xB1>(cs[j]);
        if (tt < 176) *(LDS f32x4*)(sm + L_CS + (tt >> 1) * 16) = cs;
      }
    } else {
      bar();
      ST(8);
      u32x4 af[2][4];
#pragma unroll
      for (int u = 0; u < 2; ++u)
#pragma unroll
        for (int q = 0; q < 4; ++q) af[u][q] = ldsr(sm + L_B1 + ((u * 4 + q) * 64 + l) * 16);
      if (cb != 0) {
#pragma unroll
        for (int u = 0; u < 2; ++u) {
          f32x4 zr = z4, zi = z4;
#pragma unroll
          for (int q = 0; q < 4; ++q) { zr = mm(af[u][q], R1[q], zr); zi = mm(af[u][q], I1[q], zi); }
          u32x4 o;
#pragma unroll
          for (int j = 0; j < 4; ++j) o[j] = umin(pk(zr[j], -zi[j]), POISON - 1);
          bst(o, rw, ((w * 2 + u) * 64 + l) * 16, O_X2 + cb * PST);
        }
      } else {
#pragma unroll
        for (int u = 0; u < 2; ++u) {
          f32x4 zr = z4, zi = z4;
#pragma unroll
          for (int q = 0; q < 4; ++q) { zr = mm(af[u][q], R1[q], zr); zi = mm(ldsr(sm + L_B1 + 8192 + ((u * 4 + q) * 64 + l) * 16), R1[q], zi); }
          u32x4 o;
#pragma unroll
          for (int j = 0; j < 4; ++j) o[j] = umin(pk(zr[j], zi[j]), POISON - 1);
          bst(o, rw, ((w * 2 + u) * 64 + l) * 16, O_X2);
        }
        if (t == 0) __builtin_amdgcn_raw_buffer_store_b32(tag, rw, 0, O_EP, SC1);
      }
      SB();
#pragma unroll
      for (int e = 0; e < NW1A; ++e) W1LD(e);
    }
    SB();
    ST(9);
  }

  {
    u32x4 R, I, SA, SB_, M0, M1; float tc[4], ts[4];
    int lo = l; PIN(lo);
#pragma unroll
    for (int i = 0; i < 4; ++i) {
      const int r = lo & 15, h = lo >> 4;
      const int m = 4 * h + i;
      float a = (float)((r * m) & 15) * (1.f / 16.f), c = cosr(a), sn = sinr(a);
      const u32 sg = h >= 2 ? 0x80000000u : 0u;
      R[i] = pk(c, -sn) ^ sg; I[i] = pk(sn, c) ^ sg;
      a = (float)((r * m) & 31) * (1.f / 32.f);
      u32 v = pk(2.f * cosr(a), -2.f * sinr(a));
      if (i == 0) v = h == 0 ? ((r & 1) ? 0xBF803F80u : 0x3F803F80u) : v;
      SA[i] = (i & 1) ? 0u : v; SB_[i] = (i & 1) ? v : 0u;
      a = (float)(r * m) * (1.f / 512.f); tc[i] = cosr(a); ts[i] = sinr(a);
      M0[i] = pk(2.f * c, -2.f * sn);
      a = (float)((m * (2 * r + 1)) & 31) * (1.f / 32.f);
      M1[i] = pk(2.f * cosr(a), -2.f * sinr(a));
      if (i == 0) { M0[i] = h == 0 ? 0x3F803F80u : M0[i]; M1[i] = h == 0 ? 0xBF803F80u : M1[i]; }
    }
    const u32x4 E0 = selfrag(lo & 15, lo >> 4, 0), E1 = selfrag(lo & 15, lo >> 4, 1);
    f32x4 y0[4], y1[4];
#pragma unroll
    for (int i = 0; i < 4; ++i) { y0[i] = mm(E0, xf[i], z4); y1[i] = mm(E1, xf[i], z4); PIN(y0[i]); PIN(y1[i]); PIN(tc[i]); PIN(ts[i]); }
    PIN(R); PIN(I); PIN(SA); PIN(SB_); PIN(M0); PIN(M1);
    SB();
    WAITV(0);
    bar();
    ST(10);
    u32x4 zin[4]; u32 vz = (t >> 3) * PST + (t & 7) * 16;
    #pragma unroll 1
    for (int it = 0; it < SPIN; ++it) {
      asm volatile("" : "+v"(vz) :: "memory");
#pragma unroll
      for (int i = 0; i < 4; ++i) zin[i] = bld(rw, vz, O_X2 + i * 64 * PST + g * 128);
      u32x4 m;
#pragma unroll
      for (int j = 0; j < 4; ++j) m[j] = umax(umax(umax(zin[j][0], zin[j][1]), zin[j][2]), zin[j][3]);
      if (all(clean(m))) break;
      __builtin_amdgcn_s_sleep(2);
    }
#pragma unroll
    for (int i = 0; i < 4; ++i) ldsw(sm + L_HT + (t & 7) * 4160 + ((t >> 3) & 3) * 1024 + ((16 * i + (t >> 5) + ((t & 7) >> 1)) & 63) * 16, zin[i]);
    bar();
    ST(11);
    u32x4 q[4], zs;
#pragma unroll
    for (int j = 0; j < 4; ++j) {
      q[j] = ldsr(sm + L_HT + wv * 4160 + j * 1024 + ((l + (wv >> 1)) & 63) * 16);
      zs[j] = *(LDS u32*)(sm + L_HT + wv * 4160 + j * 1024 + (16 * h + (wv >> 1)) * 16 + (r & 3) * 4);
    }
    {
      f32x4 ur[4], ui[4];
      const f32x4 sa = mm(zs, SA, z4), sb = mm(zs, SB_, z4);
      { const u32x4 z = {q[0][0], q[1][0], q[2][0], q[3][0]}; ur[0] = mm(z, R, z4); ui[0] = mm(z, I, z4); }
#pragma unroll
      for (int i = 0; i < 4; ++i) {
        if (i < 3) { const u32x4 z = {q[0][i + 1], q[1][i + 1], q[2][i + 1], q[3][i + 1]}; ur[i + 1] = mm(z, R, z4); ui[i + 1] = mm(z, I, z4); }
        SB();
        if (!job) {
#pragma unroll
          for (int e = NW1A + (NJ2 - NW1A) * i / 4; e < NW1A + (NJ2 - NW1A) * (i + 1) / 4; ++e) W1LD(e);
        }
#pragma unroll
        for (int e = NJ2 + (32 - NJ2) * i / 4; e < NJ2 + (32 - NJ2) * (i + 1) / 4; ++e) W1LD(e);
        ur[i][0] = h == 0 ? sa[i] : ur[i][0]; ui[i][0] = h == 0 ? sb[i] : ui[i][0];
        u32x4 v;
#pragma unroll
        for (int j = 0; j < 4; ++j) v[j] = pk(ur[i][j] * tc[j] - ui[i][j] * ts[j], ui[i][j] * tc[j] + ur[i][j] * ts[j]);
        y0[i] = mm(v, M0, y0[i]); y1[i] = mm(v, M1, y1[i]);
        if (i) ldsw(sm + L_HT + (4 * wv + i - 1) * 1040 + l * 16, u32x4{pk(y0[i - 1][0], y0[i - 1][1]), pk(y0[i - 1][2], y0[i - 1][3]), pk(y1[i - 1][0], y1[i - 1][1]), pk(y1[i - 1][2], y1[i - 1][3])});
      }
      ldsw(sm + L_HT + (4 * wv + 3) * 1040 + l * 16, u32x4{pk(y0[3][0], y0[3][1]), pk(y0[3][2], y0[3][3]), pk(y1[3][0], y1[3][1]), pk(y1[3][2], y1[3][3])});
    }
    bar();
    ST(12);
  }

  const int cg = wv & 3, kh = wv >> 2;
  u32x4 w2f[43];
  const u32x4 bq = *(const GLB u32x4*)((const GLB uint16_t*)b2 + 128 * jb + 8 * (t & 15));
  const u32 w2b = O_W2 + ((4 * jb + cg) * 88 + kh * 44) * 1024;
#define W2LD(i) w2f[i] = bld(rw, l * 16 + ((i) & 3) * 1024, w2b + ((i) >> 2) * 4096)
  {
    f32x4 acc[3][2];
#pragma unroll
    for (int c = 0; c < 3; ++c) { acc[c][0] = z4; acc[c][1] = z4; }
    LDS char* ap = sm + L_HT + (wv == 7 ? 16640 : 0) + r * 1040 + h * 256;
    LDS char* bp = sm + L_HT + (wv == 7 ? 0 : 16640) + r * 1040 + h * 256;
    {
      const bool gm = wv >= 6;
      u32x4 ones = {0x3F803F80u, 0x3F803F80u, 0x3F803F80u, 0x3F803F80u}; PIN(ones);
      LDS char* tp = sm + L_H + (4 * h + (r >> 2)) * 192 + (((2 * wv + ((r >> 1) & 1)) ^ (2 * (h & 1))) << 4) + (r & 1) * 8;
      u32x4 a0[2], a1[2]; u32x2 w3[2][2];
      a0[0] = ldsr(ap); a1[0] = ldsr(bp);
      w3[0][0] = ldst(tp); w3[0][1] = ldst(tp + 3072);
#pragma unroll
      for (int ks = 0; ks < 16; ++ks) {
        const int p = ks & 1;
        if (ks < 15) {
          a0[p ^ 1] = ldsr(ap + (ks + 1) * 16); a1[p ^ 1] = ldsr(bp + (ks + 1) * 16);
          w3[p ^ 1][0] = ldst(tp + (ks + 1) * 6144); w3[p ^ 1][1] = ldst(tp + (ks + 1) * 6144 + 3072);
        }
        SB();
#pragma unroll
        for (int c = 0; c < 2; ++c) { acc[c][0] = mm(wf[c][ks], a0[p], acc[c][0]); acc[c][1] = mm(wf[c][ks], a1[p], acc[c][1]); }
#pragma unroll
        for (int i = NW2A * ks / 16; i < NW2A * (ks + 1) / 16; ++i) W2LD(i);
        SB();
        if (gm) { acc[2][0] = mm(a0[p], a0[p], acc[2][0]); acc[2][1] = mm(ones, a0[p], acc[2][1]); }
        else { const u32x4 w = {w3[p][0][0], w3[p][0][1], w3[p][1][0], w3[p][1][1]}; acc[2][0] = mm(w, a0[p], acc[2][0]); acc[2][1] = mm(w, a1[p], acc[2][1]); }
      }
      if (gm) {
        const int e = r & 3;
        const float s2 = e == 0 ? acc[2][0][0] : e == 1 ? acc[2][0][1] : e == 2 ? acc[2][0][2] : acc[2][0][3];
        float mb, inv; lnstat(acc[2][1][0], s2, mb, inv);
        if (h == (r >> 2)) *(LDS f32x2*)(sm + L_ST + ((wv - 6) * 16 + r) * 8) = f32x2{mb, inv};
        if (wv == 7) {
#pragma unroll
          for (int c = 0; c < 2; ++c) { const f32x4 u = acc[c][0]; acc[c][0] = acc[c][1]; acc[c][1] = u; }
        }
      }
    }
    bar();
    ST(13);
    const f32x2 st0 = *(LDS f32x2*)(sm + L_ST + r * 8), st1 = *(LDS f32x2*)(sm + L_ST + (16 + r) * 8);
#pragma unroll
    for (int c = 0; c < 3; ++c) {
      if (c < 2 || has3) {
        const int ft = ft0 + 8 * c;
        const f32x4 cs = *(LDS f32x4*)(sm + L_CS + (wv + 8 * c) * 64 + h * 16), bb = *(LDS f32x4*)(sm + L_BB + (wv + 8 * c) * 64 + h * 16);
        float v0[4], v1[4];
#pragma unroll
        for (int j = 0; j < 4; ++j) {
          float p0 = __builtin_fmaxf((acc[c][0][j] - st0[0] * cs[j]) * st0[1] + bb[j], 0.f), p1 = __builtin_fmaxf((acc[c][1][j] - st1[0] * cs[j]) * st1[1] + bb[j], 0.f);
          v0[j] = p0 * p0; v1[j] = p1 * p1;
        }
        auto s0 = __builtin_amdgcn_permlane16_swap(pk(v0[0], v0[1]), pk(v1[0], v1[1]), false, false);
        auto s1 = __builtin_amdgcn_permlane16_swap(pk(v0[2], v0[3]), pk(v1[2], v1[3]), false, false);
        const u32x4 o = {(u32)s0[0], (u32)s1[0], (u32)s0[1], (u32)s1[1]};
        bst(o, rw, l * 16, O_HB + (g * 88 + ft) * 1024);
        ldsw(sm + L_H + (wv + 8 * c) * 1024 + l * 16, o);
      }
    }
  }
  SB();
#pragma unroll
  for (int i = NW2A; i < NW2B; ++i) W2LD(i);
  SB();
  bar();
  ST(14);

  {
    f32x16 acc = {0.f};
    LDS char* hb = sm + L_H + kh * 1024 + l * 16;
    const int tok = t >> 4, e = t & 15, d0 = 128 * jb + 8 * e;
    float res[8];
    {
      u32x4 hq[10];
#pragma unroll
      for (int i = 0; i < 10; ++i) hq[i] = ldsr(hb + i * 2048);
      LDS char* hp = sm + L_HT + tok * 1040 + (((d0 >> 2) & 3) * 16 + (d0 >> 5)) * 16 + ((d0 >> 4) & 1) * 8;
      const u32x2 h0 = *(LDS u32x2*)hp, h1 = *(LDS u32x2*)(hp + 256);
#pragma unroll
      for (int i = 0; i < 10; ++i) acc = mm32(w2f[i], hq[i], acc);
#pragma unroll
      for (int i = 0; i < 4; ++i) { const u32 hv = i < 2 ? h0[i & 1] : h1[i & 1]; res[2 * i] = blo(bq[i]) + blo(hv); res[2 * i + 1] = bhi(bq[i]) + bhi(hv); PIN(res[2 * i]); PIN(res[2 * i + 1]); }
    }
    SB();
    asm volatile("s_waitcnt vmcnt(%0)" :: "n"(NW2B - NW2A) : "memory");
    ST(15);
    u32x4 hf[9];
    const int f0 = 22 * jb + 22 + wv;
    const bool nine = jb == 3 && wv < 2;
    u32 vh = l * 16, vn = nine ? l * 16 : 0x7FFFFFF0;
    auto hload = [&]() {
      asm volatile("" : "+v"(vh), "+v"(vn) :: "memory");
#pragma unroll
      for (int i = 0; i < 8; ++i) { int f = f0 + 8 * i; f = f >= 86 ? f - 86 : f; hf[i] = bld(rw, vh, O_HB + (g * 88 + f) * 1024); }
      hf[8] = bld(rw, vn, O_HB + (g * 88 + wv) * 1024);
    };
    auto hok = [&]() {
      u32x4 m = hf[8];
#pragma unroll
      for (int i = 0; i < 8; i += 2)
#pragma unroll
        for (int j = 0; j < 4; ++j) m[j] = umax(umax(m[j], hf[i][j]), hf[i + 1][j]);
      return all(clean(m));
    };
    hload();
    SB();
#pragma unroll
    for (int i = NW2B; i < NW2C; ++i) W2LD(i);
    SB();
    if (!hok()) {
      #pragma unroll 1
    for (int it = 0; it < SPIN; ++it) {
        hload();
        if (hok()) break;
      }
    }
    SB();
#pragma unroll
    for (int i = 0; i < 8; ++i) { ldsw(sm + L_H + (22 + wv + 8 * i) * 1024 + l * 16, hf[i]); if (NW2C + i < 43) W2LD(NW2C + i); }
    if (nine) ldsw(sm + L_H + (20 + wv) * 1024 + l * 16, hf[8]);
    bar();
    ST(16);
    u32x4 hq[4];
#pragma unroll
    for (int i = 10; i < 13; ++i) hq[i & 3] = ldsr(hb + i * 2048);
#pragma unroll
    for (int i = 10; i < 43; ++i) {
      if (i + 3 < 43) hq[(i + 3) & 3] = ldsr(hb + (i + 3) * 2048);
      if (NW2C + 8 + (i - 10) < 43) W2LD(NW2C + 8 + (i - 10));
      SB();
      acc = mm32(w2f[i], hq[i & 3], acc);
      SB();
    }
#pragma unroll
    for (int q = 0; q < 4; ++q)
      *(LDS f32x4*)(sm + L_RED + (kh * 32 + (l & 31)) * 528 + (32 * cg + 8 * q + 4 * (l >> 5)) * 4) = f32x4{acc[4 * q], acc[4 * q + 1], acc[4 * q + 2], acc[4 * q + 3]};
    bar();
    ST(17);
    LDS char* rp = sm + L_RED + tok * 528 + (e >> 3) * 256 + (e & 7) * 16;
    const f32x4 a0 = *(LDS f32x4*)rp + *(LDS f32x4*)(rp + 32 * 528), a1 = *(LDS f32x4*)(rp + 128) + *(LDS f32x4*)(rp + 32 * 528 + 128);
    u32x4 o;
#pragma unroll
    for (int i = 0; i < 4; ++i) o[i] = pk(a0[i] + res[2 * i], a1[i] + res[2 * i + 1]);
    *(GLB u32x4*)((GLB uint16_t*)out + (srow0 + 32 * (tok >> 2) + (tok & 3)) * D_ + d0) = o;
  }
}

#ifdef __HIP_DEVICE_COMPILE__
#pragma clang attribute pop
#endif
extern "C" void kernel_launch(void* const* d_in, const int* in_sizes, int n_in, void* d_out, int out_size, void* d_ws, size_t ws_size, hipStream_t stream) {
  (void)hipFuncSetAttribute((const void*)k_all, hipFuncAttributeMaxDynamicSharedMemorySize, L_END);
  k_all<<<256, 512, L_END, stream>>>((const uint16_t*)d_in[0], (const uint16_t*)d_in[1], (const uint16_t*)d_in[2], (const uint16_t*)d_in[4], (const uint16_t*)d_in[5],
                                     (const uint16_t*)d_in[6], (const uint16_t*)d_in[7], (char*)d_ws, (uint16_t*)d_out);
}
```

```cpp
#include <hip/hip_runtime.h>
#include <hip/hip_bf16.h>
#include <stdint.h>

#ifdef __HIP_DEVICE_COMPILE__
#pragma clang attribute push(__attribute__((target("no-packed-fp32-ops"))), apply_to = function)
#endif
typedef unsigned u32;
typedef u32 u32x2 __attribute__((ext_vector_type(2)));
typedef u32 u32x4 __attribute__((ext_vector_type(4)));
typedef float f32x4 __attribute__((ext_vector_type(4)));
typedef float f32x2 __attribute__((ext_vector_type(2)));
typedef float f32x16 __attribute__((ext_vector_type(16)));
typedef __bf16 bf16x8 __attribute__((ext_vector_type(8)));
typedef __bf16 bf16x2 __attribute__((ext_vector_type(2)));
typedef __amdgpu_buffer_rsrc_t rsrc_t;
#define LDS __attribute__((address_space(3)))
#define GLB __attribute__((address_space(1)))
#define CST __attribute__((address_space(4)))
#define DEV __device__ __forceinline__

constexpr int S_ = 2048, D_ = 512, F_ = 1376, DF_ = 257;
constexpr u32 PST = 8448, PS1 = 12544;
constexpr u32 O_X1 = 0, O_WT = 0x320000, O_X2 = 0x540000, O_W1 = 0x760000, O_W2 = 0x8C0000,
              O_CB = 0xA20000, O_HB = 0xA40000, O_FL = 0xFC0000, O_EP = 0x1000000, WS_END = 0x1001000;
constexpr u32 POISON = 0xFFFFFFFFu;
constexpr int SC1 = 16;
constexpr int SPIN = 1 << 15;
constexpr int NJ2 = 24, NW1A = 8, NW2A = 24, NW2B = 36, NW2C = 28, NDM = 3, PRK = 7;
#define PIN(x) asm volatile("" : "+v"(x))
#define SB() __builtin_amdgcn_sched_barrier(0)
#define WAITV(n) asm volatile("s_waitcnt vmcnt(" #n ")" ::: "memory")

DEV rsrc_t mkrs(const void* p, u32 bytes) { return __builtin_amdgcn_make_buffer_rsrc((void*)p, 0, bytes, 0x00020000); }
DEV u32 pk(float a, float b) { f32x2 v = {a, b}; return __builtin_bit_cast(u32, __builtin_convertvector(v, bf16x2)); }
DEV float blo(u32 v) { return __builtin_bit_cast(float, v << 16); }
DEV float bhi(u32 v) { return __builtin_bit_cast(float, v & 0xFFFF0000u); }
DEV float rbf(float a) { return blo(pk(a, a)); }
DEV float cosr(float rev) { return __builtin_amdgcn_cosf(rev); }
DEV float sinr(float rev) { return __builtin_amdgcn_sinf(rev); }
DEV f32x4 mm(u32x4 a, u32x4 b, f32x4 c) { return __builtin_amdgcn_mfma_f32_16x16x32_bf16(__builtin_bit_cast(bf16x8, a), __builtin_bit_cast(bf16x8, b), c, 0, 0, 0); }
DEV f32x16 mm32(u32x4 a, u32x4 b, f32x16 c) { return __builtin_amdgcn_mfma_f32_32x32x16_bf16(__builtin_bit_cast(bf16x8, a), __builtin_bit_cast(bf16x8, b), c, 0, 0, 0); }
template <int C> DEV float dpp(float x) { return __builtin_bit_cast(float, __builtin_amdgcn_mov_dpp(__builtin_bit_cast(int, x), C, 0xf, 0xf, true)); }
DEV float wsum(float v) {
  v += dpp<0xB1>(v); v += dpp<0x4E>(v); v += dpp<0x141>(v); v += dpp<0x128>(v);
  u32 u = __builtin_bit_cast(u32, v);
  auto a = __builtin_amdgcn_permlane16_swap(u, u, false, false);
  v = __builtin_bit_cast(float, (u32)a[0]) + __builtin_bit_cast(float, (u32)a[1]);
  u = __builtin_bit_cast(u32, v);
  auto b = __builtin_amdgcn_permlane32_swap(u, u, false, false);
  return __builtin_bit_cast(float, (u32)b[0]) + __builtin_bit_cast(float, (u32)b[1]);
}
DEV u32x4 ldsr(LDS char* p) { return *(LDS u32x4*)p; }
DEV void ldsw(LDS char* p, u32x4 v) { *(LDS u32x4*)p = v; }
DEV u32 umax(u32 a, u32 b) { return a > b ? a : b; }
DEV u32 umin(u32 a, u32 b) { return a < b ? a : b; }
DEV bool all(bool p) { return __builtin_amdgcn_ballot_w64(!p) == 0; }
DEV void bar() { asm volatile("s_waitcnt lgkmcnt(0)" ::: "memory"); __builtin_amdgcn_s_barrier(); asm volatile("" ::: "memory"); }
DEV void lnstat(float s1, float s2, float& mb, float& inv) {
  float m = s1 * (1.f / 512.f);
  float v = __builtin_fmaxf(s2 * (1.f / 512.f) - m * m, 0.f);
  mb = rbf(m);
  inv = __builtin_amdgcn_rcpf(rbf(__builtin_amdgcn_sqrtf(rbf(rbf(v) + 1e-5f))));
}

DEV float half_sum(float c) {
  u32 u = __builtin_bit_cast(u32, c);
  auto a = __builtin_amdgcn_permlane16_swap(u, u, false, false);
  c = __builtin_bit_cast(float, (u32)a[0]) + __builtin_bit_cast(float, (u32)a[1]);
  u = __builtin_bit_cast(u32, c);
  auto b = __builtin_amdgcn_permlane32_swap(u, u, false, false);
  return __builtin_bit_cast(float, (u32)b[0]) + __builtin_bit_cast(float, (u32)b[1]);
}
DEV u32x4 bld(rsrc_t w, u32 v, u32 s) { return __builtin_amdgcn_raw_buffer_load_b128(w, v, s, SC1); }
DEV void bst(u32x4 d, rsrc_t w, u32 v, u32 s) { __builtin_amdgcn_raw_buffer_store_b128(d, w, v, s, SC1); asm volatile("s_nop 1" :: "v"(d) : "memory"); }
DEV void dma(rsrc_t w, u32 lds, u32 v, u32 s) { asm volatile("s_mov_b32 m0, %0\n\ts_nop 0\n\tbuffer_load_dwordx4 %1, %2, %3 offen sc1 lds" :: "s"(lds), "v"(v), "s"(w), "s"(s) : "memory"); }
DEV void dmap(rsrc_t w, u32 lds, u32 v, u32 s) { asm volatile("s_mov_b32 m0, %0\n\ts_nop 0\n\tbuffer_load_dwordx4 %1, %2, %3 offen lds" :: "s"(lds), "v"(v), "s"(w), "s"(s) : "memory"); }
DEV void touchp(rsrc_t w, u32 lds, u32 v, u32 s) { asm volatile("s_mov_b32 m0, %0\n\ts_nop 0\n\tbuffer_load_dword %1, %2, %3 offen lds" :: "s"(lds), "v"(v), "s"(w), "s"(s) : "memory"); }
DEV void mma(f32x4& c, u32x4 a, u32x4 b, bool first) {
  if (first) asm volatile("v_mfma_f32_16x16x32_bf16 %0, %1, %2, 0" : "=&v"(c) : "v"(a), "v"(b));
  else asm volatile("v_mfma_f32_16x16x32_bf16 %0, %1, %2, %0" : "+v"(c) : "v"(a), "v"(b));
}
DEV u32x4 ald4(rsrc_t w, u32 v, u32 s) { u32x4 d; asm volatile("buffer_load_dwordx4 %0, %1, %2, %3 offen" : "=v"(d) : "v"(v), "s"(w), "s"(s) : "memory"); return d; }
DEV u32 ald1(rsrc_t w, u32 v, u32 s) { u32 d; asm volatile("buffer_load_dword %0, %1, %2, %3 offen" : "=v"(d) : "v"(v), "s"(w), "s"(s) : "memory"); return d; }
DEV u32x2 ald2(const void* p, u32 v) { u32x2 d; asm volatile("global_load_dwordx2 %0, %1, %2" : "=v"(d) : "v"(v), "s"(p) : "memory"); return d; }
typedef short s16x4 __attribute__((ext_vector_type(4)));
DEV u32x2 ldst(LDS char* p) { return __builtin_bit_cast(u32x2, __builtin_amdgcn_ds_read_tr16_b64_v4i16((LDS s16x4*)p)); }
template <int N> struct IC { static constexpr int v = N; };
DEV bool clean(u32x4 a) { return umax(umax(a[0], a[1]), umax(a[2], a[3])) != POISON; }

constexpr int L_H = 0;
constexpr int L_RED = 90112;
constexpr int L_B0 = 98304, L_B1 = L_B0 + 8192;
constexpr int L_HT = 123904;
constexpr int L_CC = L_HT + 9216 + 8192, L_CA = L_HT + 9216;
constexpr int L_ST = L_HT + 33280;
constexpr int L_CS = L_ST + 256, L_BB = L_CS + 1408;
constexpr int L_FL = L_BB + 1408, L_DUM = L_FL + 256, L_END = L_DUM + 256;

__global__ __launch_bounds__(512, 2) void k_all(const uint16_t* __restrict__ x, const uint16_t* __restrict__ wr, const uint16_t* __restrict__ wi,
                                                const uint16_t* __restrict__ w1, const uint16_t* __restrict__ b1, const uint16_t* __restrict__ w2,
                                                const uint16_t* __restrict__ b2, char* ws, uint16_t* __restrict__ out) {
  extern __shared__ __attribute__((aligned(16))) char smem_[];
  LDS char* sm = (LDS char*)smem_;
  asm volatile("" : "+s"(x), "+s"(wr), "+s"(wi), "+s"(w1), "+s"(b1), "+s"(w2), "+s"(b2), "+s"(ws), "+s"(out));
  const int t = threadIdx.x, l = t & 63, wv = __builtin_amdgcn_readfirstlane(t >> 6), r = l & 15, h = l >> 4;
  const int bid = blockIdx.x;
  const int cb = (bid & 7) * 32 + (bid >> 3), g = cb >> 2, jb = cb & 3;
  const rsrc_t rw = mkrs(ws, WS_END);
  const f32x4 z4 = {0.f, 0.f, 0.f, 0.f};
  const u32x4 P4 = {POISON, POISON, POISON, POISON};
  const int srow0 = 512 * (g >> 4) + 256 * (g & 1) + 16 * ((g >> 3) & 1) + 4 * ((g >> 1) & 3);
  const int ft0 = 22 * jb + wv;
  const bool has3 = wv < 6 && ft0 + 16 < 86;
  const u32 tag = *(const CST u32*)(ws + O_EP) + 1u;
  const int i0q = (t * 21846) >> 16, i0p = t - 3 * i0q, i1q = ((t + 512) * 21846) >> 16, i1p = t + 512 - 3 * i1q;
  u32x4 xf[4]; u32 bv;

  auto w3dma = [&](int g0, int gs, auto n) {
    const rsrc_t r1 = mkrs(w1, D_ * F_ * 2);
    const u32 va = (l >> 3) * (F_ * 2) + ((l ^ (l >> 3 & 6)) & 7) * 16, vb = (l >> 2) * (F_ * 2) + ((l ^ (l >> 3 & 2)) & 3) * 16;
#pragma unroll
    for (int i = 0; i < n.v; ++i) {
      const int G = g0 + gs * i; const u32 so = G * (16 * F_ * 2) + 704 * jb + 512;
      dmap(r1, L_H + G * 3072, va, so); dmap(r1, L_H + G * 3072 + 1024, va, so + 8 * F_ * 2); dmap(r1, L_H + G * 3072 + 2048, vb, so + (jb == 3 ? 0 : 128));
    }
  };
  {
    const rsrc_t rx = mkrs(x, S_ * D_ * 2);
    u32x4 xa = ald4(rx, 64 * r + 16 * h, (256 * (bid >> 5) + 32 * wv + (bid & 31)) * (D_ * 2));
    const int ksr = ((bid & 63) + 64 * (8 * (bid >> 6) + wv)) * DF_;
    u32x2 wa = ald2(wr + ksr, 8 * l), wb = ald2(wi + ksr, 8 * l);
    u32 na = ((const CST u32*)wr)[(ksr >> 1) + 128], nb = ((const CST u32*)wi)[(ksr >> 1) + 128];
    SB();
    u32 v[8];
    const int id = (wv < 3 ? wv : wv - 3) * 256 + bid, tp = id >> 4;
    const bool t1 = wv < 3 && id < 688, t2 = wv >= 3 && id < 688, nat = tp - 11 * ((tp * 47) >> 9) >= 8;
    if (t1) {
      const rsrc_t rt = mkrs(w1, D_ * F_ * 2);
      const u32 so = (id & 15) * (64 * F_) + 64 * tp, vo = h * (8 * F_) + 4 * r;
#pragma unroll
      for (int j = 0; j < 8; ++j) v[j] = ald1(rt, vo, so + ((j >> 2) * 16 + (j & 3)) * (F_ * 2));
    } else if (t2) {
      const rsrc_t rt = mkrs(w2, D_ * F_ * 2);
      const int sh = nat ? 10 : 11;
      const u32 so = tp * 32768 + ((id & 8) << (nat ? 11 : 7)) + 128 * (id & 7), vo = ((l >> 5) << (sh + 3)) + 4 * (l & 31);
#pragma unroll
      for (int j = 0; j < 8; ++j) v[j] = ald1(rt, vo, so + (j << sh));
    } else if (wv == 7) {
      unsigned long long pc; u32 len;
      asm volatile("s_getpc_b64 %0\n.Lpf_a:\n\ts_mov_b32 %1, .Lpf_b-.Lpf_a" : "=s"(pc), "=s"(len));
      const rsrc_t rc = mkrs((const void*)pc, len);
#pragma unroll
      for (int i = 0; i < 3; ++i) touchp(rc, L_DUM, l * 128, i * 8192);
    }
    SB();
    {
      const int i = wv & 3, n2 = 4 * h + i;
      LDS char* cp = sm + L_CA + l * 16 + i * 4;
      *(LDS u32*)(cp + (L_CC - L_CA) + 8192 + (wv >> 2) * 1024) = (r >> 1) + 8 * (wv >> 2) == n2 ? 0x3F80u << (16 * (r & 1)) : 0u;
      if (wv < 4) {
        const float a0 = (float)((2 * n2 * r) & 31) * (1.f / 32.f), a1 = (float)(((2 * n2 + 1) * r) & 31) * (1.f / 32.f);
        *(LDS u32*)cp = pk(cosr(a0), cosr(a1));
        *(LDS u32*)(cp + 1024) = r == 0 ? 0xBF803F80u : pk(-sinr(a0), -sinr(a1));
        const float at = (float)(r * n2) * (1.f / 512.f);
        *(LDS float*)(cp + 2048) = cosr(at); *(LDS float*)(cp + 3072) = sinr(at);
      } else {
        const float ph = (float)((r * n2) & 15) * (1.f / 16.f), c = cosr(ph), sn = sinr(ph);
        *(LDS u32*)(cp + 4096) = pk(c, sn); *(LDS u32*)(cp + 5120) = pk(-sn, c) ^ (r >= 8 ? 0x80008000u : 0u);
        const float ps = (float)((r * n2) & 31) * (1.f / 32.f);
        const u32 cs = pk(cosr(ps), r == 0 ? ((i & 1) ? -1.f : 1.f) : -sinr(ps));
        *(LDS u32*)(cp + 6144) = (r & 1) ? cs << 16 : cs & 0xFFFFu;
        *(LDS u32*)(cp + 7168) = (r & 1) ? cs & 0xFFFF0000u : cs >> 16;
      }
    }
    const int k1 = (t >> 3) & 15, k2 = ((t >> 5) & 12) | ((t >> 1) & 3), ka = k1 + 32 * k2;
    LDS char* kp = sm + L_B1 + (t & 1) * 4160 + (k1 ? (wv < 4 ? ka : 512 - ka) : 16 * k2) * 4;
    PIN(kp);
    SB();
    bst(P4, rw, t * 16, O_X2 + cb * PST);
#pragma unroll
    for (int i = 0; i < 4; ++i) xf[i] = ald4(rx, 64 * r + 16 * h, (srow0 + 32 * wv + i) * (D_ * 2));
    SB();
    asm volatile("s_waitcnt vmcnt(%11)" : "+v"(xa), "+v"(wa), "+v"(wb), "+v"(v[0]), "+v"(v[1]), "+v"(v[2]), "+v"(v[3]), "+v"(v[4]), "+v"(v[5]), "+v"(v[6]), "+v"(v[7])
                 : "n"(5) : "memory");
    asm volatile("" : "+s"(na), "+s"(nb));
    ldsw(sm + L_B1 + wv * 1040 + l * 16, u32x4{__builtin_amdgcn_perm(wb[0], wa[0], 0x05040100u), __builtin_amdgcn_perm(wb[0], wa[0], 0x07060302u),
                                               __builtin_amdgcn_perm(wb[1], wa[1], 0x05040100u), __builtin_amdgcn_perm(wb[1], wa[1], 0x07060302u)});
    *(LDS u32*)(sm + L_B1 + wv * 1040 + 1024) = (ksr & 1) ? (na >> 16) | (nb & 0xFFFF0000u) : (na & 0xFFFFu) | (nb << 16);
    u32x4 p0, p1;
#pragma unroll
    for (int i = 0; i < 4; ++i) { p0[i] = __builtin_amdgcn_perm(v[2 * i + 1], v[2 * i], 0x05040100u); p1[i] = __builtin_amdgcn_perm(v[2 * i + 1], v[2 * i], 0x07060302u); }
    if (t1) {
      const u32 o = O_W1 + ((id >> 4) * 32 + (id & 15)) * 1024;
      if (!nat) { bst(p0, rw, l * 16, o); bst(p1, rw, l * 16, o + 16384); }
      const u32x4 ones = {0x3F803F80u, 0x3F803F80u, 0x3F803F80u, 0x3F803F80u};
      const float c0 = mm(ones, p0, z4)[0], c1 = mm(ones, p1, z4)[0];
      if (l < 32) __builtin_amdgcn_raw_buffer_store_b32(__builtin_bit_cast(u32, l < 16 ? c0 : c1), rw, nat ? 8 * r + 4 * h : l * 4, O_CB + (id & 15) * 5632 + (id >> 4) * 128, SC1);
    }
    bar();
    const f32x4 d0 = mm(xa, ldsr(sm + L_CC + 8192 + l * 16), z4), d1 = mm(xa, ldsr(sm + L_CC + 9216 + l * 16), z4);
    { u32x4 q;
#pragma unroll
      for (int i = 0; i < 4; ++i) q[i] = *(LDS u32*)(kp + i * 1040);
      bst(q, rw, t * 16, O_WT + bid * PST); }
    LDS char* cq = sm + L_CA + l * 16;
    const u32x4 C1 = ldsr(cq), S1 = ldsr(cq + 1024), R2 = ldsr(cq + 4096), I2 = ldsr(cq + 5120), SR = ldsr(cq + 6144), SI = ldsr(cq + 7168);
    const f32x4 tc = *(LDS f32x4*)(cq + 2048), ts = *(LDS f32x4*)(cq + 3072);
    if (wv == 7) {
      u32x4 q;
#pragma unroll
      for (int i = 0; i < 4; ++i) q[i] = *(LDS u32*)(sm + L_B1 + (4 * (t & 1) + i) * 1040 + 1024);
      if (l < 2) bst(q, rw, 8192 + l * 16, O_WT + bid * PST);
    }
    SB();
    float s1 = 0.f, s2 = 0.f;
#pragma unroll
    for (int j = 0; j < 4; ++j) { s1 += d0[j] + d1[j]; s2 = __builtin_fmaf(d0[j], d0[j], __builtin_fmaf(d1[j], d1[j], s2)); }
    s1 = wsum(s1); s2 = wsum(s2);
    float mb, inv; lnstat(s1, s2, mb, inv);
    u32x4 a;
#pragma unroll
    for (int j = 0; j < 4; ++j) a[j] = pk((d0[j] - mb) * inv, (d1[j] - mb) * inv);
    f32x4 yr = mm(a, C1, z4), yi = mm(a, S1, z4);
    u32x4 b;
#pragma unroll
    for (int j = 0; j < 4; ++j) b[j] = pk(yr[j] * tc[j] + yi[j] * ts[j], yi[j] * tc[j] - yr[j] * ts[j]);
    f32x4 xr = mm(R2, b, z4), xi = mm(I2, b, z4), sr = mm(SR, b, z4), si = mm(SI, b, z4);
    u32x4 o;
#pragma unroll
    for (int j = 0; j < 4; ++j) o[j] = pk(r == 0 ? sr[j] : xr[j], r == 0 ? si[j] : xi[j]);
    ldsw(sm + L_HT + wv * 1040 + l * 16, o);
    if (t < 2) *(LDS u32*)(sm + L_FL + t * 4) = 0u;
    WAITV(0);
    PIN(xf[0]); PIN(xf[1]); PIN(xf[2]); PIN(xf[3]);
    bar();
    { u32x4 q; q[3] = tag;
#pragma unroll
      for (int j = 0; j < 3; ++j) q[j] = *(LDS u32*)(sm + L_HT + (3 * i0p + j) * 1040 + i0q * 4);
      bst(q, rw, t * 16, O_X1 + bid * PS1);
      if (wv < 4) {
#pragma unroll
        for (int j = 0; j < 3; ++j) q[j] = *(LDS u32*)(sm + L_HT + (3 * i1p + j) * 1040 + i1q * 4);
        bst(q, rw, t * 16 + 8192, O_X1 + bid * PS1);
      } }
    SB();
    if (t2) {
      int n = tp - 11 * ((id & 7) >> 1); n = n < 0 ? n + 43 : n;
      const u32 o2 = O_W2 + ((id & 7) * 176 + ((id >> 3) & 1) * 44 + n) * 1024;
      bst(p0, rw, l * 16, o2); bst(p1, rw, l * 16, o2 + 88 * 1024);
    }
    SB();
  }
  if (wv >= 4) {
    w3dma(wv - 4, 4, IC<NDM>());
    const int i = wv & 3, m = 4 * h + i;
    LDS char* cp = sm + L_CC + l * 16 + i * 4;
    float a = (float)((r * m) & 15) * (1.f / 16.f), c = cosr(a), sn = sinr(a);
    const u32 sg = h >= 2 ? 0x80000000u : 0u;
    *(LDS u32*)cp = pk(c, -sn) ^ sg; *(LDS u32*)(cp + 1024) = pk(sn, c) ^ sg;
    *(LDS u32*)(cp + 4096) = m == 0 ? 0x3F803F80u : pk(2.f * c, -2.f * sn);
    a = (float)((r * m) & 31) * (1.f / 32.f);
    const u32 v = m == 0 ? ((r & 1) ? 0xBF803F80u : 0x3F803F80u) : pk(2.f * cosr(a), -2.f * sinr(a));
    *(LDS u32*)(cp + 2048) = (i & 1) ? 0u : v; *(LDS u32*)(cp + 3072) = (i & 1) ? v : 0u;
    a = (float)((m * (2 * r + 1)) & 31) * (1.f / 32.f);
    *(LDS u32*)(cp + 5120) = m == 0 ? 0xBF803F80u : pk(2.f * cosr(a), -2.f * sinr(a));
    a = (float)(r * m) * (1.f / 512.f);
    *(LDS float*)(cp + 6144) = cosr(a); *(LDS float*)(cp + 7168) = sinr(a);
  }
  SB();

  u32x4 wf[2][16];
  const u32 w1b = O_W1 + ft0 * 16384;
#define W1LD(e) wf[(e) & 1][(e) >> 1] = bld(rw, l * 16 + (((e) >> 1) & 3) * 1024, w1b + ((e) & 1) * 131072 + ((e) >> 3) * 4096)
  const int job = wv >> 2;
  {
    const int w = wv & 3;
    const bool act = job == 0 || cb == 0;
    u32x4 wt[2], R1[4], I1[4], R2[2][2], I2[2][2];
    float tc[2][4], ts[2][4], uc[2][4], us[2][4];
    if (act) {
      int lo = l; PIN(lo);
      const int r = lo & 15, h = lo >> 4;
      const int k1 = 16 * w + r;
      const u32 odd = (r & 1) ? 0x80008000u : 0u, two = (r & 2) ? 0x80008000u : 0u;
#pragma unroll
      for (int i = 0; i < 4; ++i) {
        const int m = 4 * h + i;
        float a = (float)((k1 * m) & 63) * (1.f / 64.f), c = cosr(a), sn = sinr(a);
        u32 R = pk(c, sn), I = pk(-sn, c);
        R1[0][i] = R; I1[0][i] = I; R1[2][i] = R ^ odd; I1[2][i] = I ^ odd;
        u32 Rq = ((r & 1) ? I : R) ^ two, Iq = ((r & 1) ? R : I) ^ two ^ odd;
        R1[1][i] = Rq; I1[1][i] = Iq; R1[3][i] = Rq ^ odd; I1[3][i] = Iq ^ odd;
        a = (float)((r * m) & 31) * (1.f / 32.f); c = cosr(a); sn = sinr(a);
        R = pk(c, sn); I = pk(-sn, c);
        const u32 fi = (i & 1) ? 0x80008000u : 0u;
        R2[0][0][i] = R; I2[0][0][i] = I; R2[0][1][i] = R ^ odd; I2[0][1][i] = I ^ odd;
        R2[1][0][i] = R ^ fi; I2[1][0][i] = I ^ fi; R2[1][1][i] = R ^ odd ^ fi; I2[1][1][i] = I ^ odd ^ fi;
#pragma unroll
        for (int u = 0; u < 2; ++u) {
          a = (float)(k1 * (16 * u + m)) * (1.f / 2048.f); tc[u][i] = cosr(a) * 0x1p-20f; ts[u][i] = sinr(a) * 0x1p-20f;
          a = (float)((16 * w + m) * (16 * u + r)) * (1.f / 2048.f); uc[u][i] = cosr(a); us[u][i] = sinr(a);
          PIN(tc[u][i]); PIN(ts[u][i]); PIN(uc[u][i]); PIN(us[u][i]);
        }
      }
#pragma unroll
      for (int i = 0; i < 4; ++i) { PIN(R1[i]); PIN(I1[i]); PIN(R2[i >> 1][i & 1]); PIN(I2[i >> 1][i & 1]); }
    }
    u32 vt = ((h >> 1) * 64 + 16 * w + r) * PST + (h & 1) * 16, vb = umin(t >= 256 ? t : (t & ~31) + 2 * r + (h & 1), jb == 3 ? 319 : 351) * 2;
    PIN(vt); PIN(vb);
    SB();
    WAITV(0);
    {
      const int tt = t - 256, jq = ((tt + 512) * 21846) >> 16, jp = tt + 512 - 3 * jq;
      u32x4 g0, g1; u32 v0 = i0q * PS1 + i0p * 16, v1 = jq * PS1 + jp * 16;
      #pragma unroll 1
    for (int it = 0; it < SPIN; ++it) {
        asm volatile("" : "+v"(v0), "+v"(v1) :: "memory");
        g0 = bld(rw, v0, O_X1 + cb * 48);
        bool ok;
        if (job) { g1 = bld(rw, v1, O_X1 + cb * 48); ok = (g0[3] == tag) & (g1[3] == tag); } else ok = g0[3] == tag;
        if (all(ok)) break;
        __builtin_amdgcn_s_sleep(2);
      }
      LDS char* p = sm + L_B0 + i0q * 32 + i0p * 12;
      *(LDS u32*)p = g0[0]; *(LDS u32*)(p + 4) = g0[1]; if (i0p != 2) *(LDS u32*)(p + 8) = g0[2];
      if (job) { p = sm + L_B0 + jq * 32 + jp * 12; *(LDS u32*)p = g1[0]; *(LDS u32*)(p + 4) = g1[1]; if (jp != 2) *(LDS u32*)(p + 8) = g1[2]; }
    }
    if (cb == 0) {
      bar();
      const u32x4 a = ldsr(sm + L_B0 + t * 16);
      ldsw(sm + L_B0 + t * 16, a & 0xFFFFu); ldsw(sm + L_HT + t * 16, a >> 16);
    }
    bar();
    if (act) {
#pragma unroll
      for (int u = 0; u < 2; ++u) wt[u] = bld(rw, vt + u * 128 * PST, O_WT + (job ? 8192 : cb * 32));
    } else __builtin_amdgcn_s_sleep(1);
    SB();
    bv = __builtin_amdgcn_raw_buffer_load_b16(mkrs(b1, F_ * 2), vb, 704 * jb, 0);
    SB();
    if (act) {
      u32x4 bf[2], af[2][4];
      LDS char* ap = sm + (job ? L_HT : L_B0) + ((h >> 1) * 32 + r) * 32 + (h & 1) * 16;
#pragma unroll
      for (int u = 0; u < 2; ++u)
#pragma unroll
        for (int q = 0; q < 4; ++q) af[u][q] = ldsr(ap + q * 2048 + u * 512);
      SB();
#pragma unroll
      for (int u = 0; u < 2; ++u) {
        f32x4 yr = z4, yi = z4;
#pragma unroll
        for (int q = 0; q < 4; ++q) { yr = mm(af[u][q], R1[q], yr); yi = mm(af[u][q], I1[q], yi); }
#pragma unroll
        for (int j = 0; j < 4; ++j) bf[u][j] = pk(yr[j] * tc[u][j] + yi[j] * ts[u][j], yi[j] * tc[u][j] - yr[j] * ts[u][j]);
      }
      u32x4 ef[2];
#pragma unroll
      for (int u = 0; u < 2; ++u) {
        f32x4 xr = mm(R2[u][1], bf[1], mm(R2[u][0], bf[0], z4)), xi = mm(I2[u][1], bf[1], mm(I2[u][0], bf[0], z4));
#pragma unroll
        for (int j = 0; j < 4; ++j) { float a = blo(wt[u][j]), b = bhi(wt[u][j]); ef[u][j] = pk(xr[j] * a - xi[j] * b, -(xr[j] * b + xi[j] * a)); }
      }
#pragma unroll
      for (int u = 0; u < 2; ++u) {
        f32x4 yr = mm(ef[1], R2[u][1], mm(ef[0], R2[u][0], z4)), yi = mm(ef[1], I2[u][1], mm(ef[0], I2[u][0], z4));
        u32x4 o;
#pragma unroll
        for (int j = 0; j < 4; ++j) o[j] = pk(yr[j] * uc[u][j] + yi[j] * us[u][j], yi[j] * uc[u][j] - yr[j] * us[u][j]);
        ldsw(sm + L_B1 + job * 8192 + ((u * 4 + w) * 64 + l) * 16, o);
      }
      if (l == 0) __hip_atomic_fetch_add((LDS u32*)(sm + L_FL + 4), 1u, __ATOMIC_RELAXED, __HIP_MEMORY_SCOPE_WORKGROUP);
    }
    if (job) {
#pragma unroll
      for (int c = 0; c < 6; ++c) if (w + 4 * c < 22) bst(P4, rw, l * 16, O_HB + (g * 88 + 22 * jb + w + 4 * c) * 1024);
      w3dma(4 * NDM + w, 4, IC<8 - NDM>());
      const int tt = t - 256;
      u32x4 cp[8];
      if (w < 3) {
#pragma unroll
        for (int i = 0; i < 8; ++i) cp[i] = bld(rw, ((tt & 1) * 8 + i) * 5632 + (tt >> 1) * 16, O_CB + jb * 1408);
      }
      SB();
#pragma unroll
      for (int e = 0; e < NJ2; ++e) W1LD(e);
      SB();
      if (w < 3) {
        f32x4 cs = z4;
#pragma unroll
        for (int i = 0; i < 8; ++i) cs += __builtin_bit_cast(f32x4, cp[i]);
#pragma unroll
        for (int j = 0; j < 4; ++j) cs[j] += dpp<0xB1>(cs[j]);
        if (tt < 176) *(LDS f32x4*)(sm + L_CS + (tt >> 1) * 16) = cs;
      }
    } else {
#pragma unroll 1
      for (int it = 0; it < SPIN; ++it) {
        asm volatile("" ::: "memory");
        if (__builtin_amdgcn_readfirstlane(*(volatile LDS u32*)(sm + L_FL + 4)) == (cb ? 4 : 8)) break;
      }
      asm volatile("" ::: "memory");
      u32x4 af[2][4];
#pragma unroll
      for (int u = 0; u < 2; ++u)
#pragma unroll
        for (int q = 0; q < 4; ++q) af[u][q] = ldsr(sm + L_B1 + ((u * 4 + q) * 64 + l) * 16);
      if (cb != 0) {
#pragma unroll
        for (int u = 0; u < 2; ++u) {
          f32x4 zr = z4, zi = z4;
#pragma unroll
          for (int q = 0; q < 4; ++q) { zr = mm(af[u][q], R1[q], zr); zi = mm(af[u][q], I1[q], zi); }
          u32x4 o;
#pragma unroll
          for (int j = 0; j < 4; ++j) o[j] = umin(pk(zr[j], -zi[j]), POISON - 1);
          bst(o, rw, ((w * 2 + u) * 64 + l) * 16, O_X2 + cb * PST);
        }
      } else {
#pragma unroll
        for (int u = 0; u < 2; ++u) {
          f32x4 zr = z4, zi = z4;
#pragma unroll
          for (int q = 0; q < 4; ++q) { zr = mm(af[u][q], R1[q], zr); zi = mm(ldsr(sm + L_B1 + 8192 + ((u * 4 + q) * 64 + l) * 16), R1[q], zi); }
          u32x4 o;
#pragma unroll
          for (int j = 0; j < 4; ++j) o[j] = umin(pk(zr[j], zi[j]), POISON - 1);
          bst(o, rw, ((w * 2 + u) * 64 + l) * 16, O_X2);
        }
        if (t == 0) __builtin_amdgcn_raw_buffer_store_b32(tag, rw, 0, O_EP, SC1);
      }
      SB();
#pragma unroll
      for (int e = 0; e < NW1A; ++e) W1LD(e);
    }
    SB();
  }

  LDS char* ap, *tp;
  {
    LDS char* cq = sm + L_CC + l * 16;
    const u32x4 E0 = ldsr(cq + 8192), E1 = ldsr(cq + 9216);
    u32x4 R = ldsr(cq), I = ldsr(cq + 1024), SA = ldsr(cq + 2048), SB_ = ldsr(cq + 3072), M0 = ldsr(cq + 4096), M1 = ldsr(cq + 5120);
    f32x4 tc = *(LDS f32x4*)(cq + 6144), ts = *(LDS f32x4*)(cq + 7168);
    if (t < 352) *(LDS u32*)(sm + L_BB + t * 4) = bv << 16;
    f32x4 y0[4], y1[4];
#pragma unroll
    for (int i = 0; i < 4; ++i) { y0[i] = mm(E0, xf[i], z4); y1[i] = mm(E1, xf[i], z4); PIN(y0[i]); PIN(y1[i]); }
    PIN(tc); PIN(ts);
    ap = sm + L_HT + r * 1040 + h * 256;
    tp = sm + L_H + (job ? 2048 + (l >> 2) * 64 + ((wv ^ h) & 1) * 32 : (l >> 2) * 128 + ((wv ^ (l >> 3)) & 3) * 32) + (l & 3) * 8;
    PIN(ap); PIN(tp);
    PIN(R); PIN(I); PIN(SA); PIN(SB_); PIN(M0); PIN(M1);
    SB();
    WAITV(0);
    bar();
    if (t == 0) __builtin_amdgcn_raw_buffer_store_b32(tag, rw, 0, O_FL + cb * 128, SC1);
    u32x4 zin[4]; u32 vz = (t >> 3) * PST + (t & 7) * 16;
    #pragma unroll 1
    for (int it = 0; it < SPIN; ++it) {
      asm volatile("" : "+v"(vz) :: "memory");
#pragma unroll
      for (int i = 0; i < 4; ++i) zin[i] = bld(rw, vz, O_X2 + i * 64 * PST + g * 128);
      u32x4 m;
#pragma unroll
      for (int j = 0; j < 4; ++j) m[j] = umax(umax(umax(zin[j][0], zin[j][1]), zin[j][2]), zin[j][3]);
      if (all(clean(m))) break;
      __builtin_amdgcn_s_sleep(2);
    }
#pragma unroll
    for (int i = 0; i < 4; ++i) ldsw(sm + L_HT + (t & 7) * 4160 + ((t >> 3) & 3) * 1024 + ((16 * i + (t >> 5) + ((t & 7) >> 1)) & 63) * 16, zin[i]);
    bar();
    u32x4 q[4], zs;
#pragma unroll
    for (int j = 0; j < 4; ++j) {
      q[j] = ldsr(sm + L_HT + wv * 4160 + j * 1024 + ((l + (wv >> 1)) & 63) * 16);
      zs[j] = *(LDS u32*)(sm + L_HT + wv * 4160 + j * 1024 + (16 * h + (wv >> 1)) * 16 + (r & 3) * 4);
    }
    {
      f32x4 ur[4], ui[4];
      const f32x4 sa = mm(zs, SA, z4), sb = mm(zs, SB_, z4);
      { const u32x4 z = {q[0][0], q[1][0], q[2][0], q[3][0]}; ur[0] = mm(z, R, z4); ui[0] = mm(z, I, z4); }
#pragma unroll
      for (int i = 0; i < 4; ++i) {
        if (i < 3) { const u32x4 z = {q[0][i + 1], q[1][i + 1], q[2][i + 1], q[3][i + 1]}; ur[i + 1] = mm(z, R, z4); ui[i + 1] = mm(z, I, z4); }
        SB();
        if (!job) {
#pragma unroll
          for (int e = NW1A + (NJ2 - NW1A) * i / 4; e < NW1A + (NJ2 - NW1A) * (i + 1) / 4; ++e) W1LD(e);
        }
#pragma unroll
        for (int e = NJ2 + (32 - NJ2) * i / 4; e < NJ2 + (32 - NJ2) * (i + 1) / 4; ++e) W1LD(e);
        ur[i][0] = h == 0 ? sa[i] : ur[i][0]; ui[i][0] = h == 0 ? sb[i] : ui[i][0];
        u32x4 v;
#pragma unroll
        for (int j = 0; j < 4; ++j) v[j] = pk(ur[i][j] * tc[j] - ui[i][j] * ts[j], ui[i][j] * tc[j] + ur[i][j] * ts[j]);
        y0[i] = mm(v, M0, y0[i]); y1[i] = mm(v, M1, y1[i]);
        if (i) ldsw(sm + L_HT + (4 * wv + i - 1) * 1040 + l * 16, u32x4{pk(y0[i - 1][0], y0[i - 1][1]), pk(y0[i - 1][2], y0[i - 1][3]), pk(y1[i - 1][0], y1[i - 1][1]), pk(y1[i - 1][2], y1[i - 1][3])});
      }
      ldsw(sm + L_HT + (4 * wv + 3) * 1040 + l * 16, u32x4{pk(y0[3][0], y0[3][1]), pk(y0[3][2], y0[3][3]), pk(y1[3][0], y1[3][1]), pk(y1[3][2], y1[3][3])});
    }
    bar();
  }

  const int cg = wv & 3, kh = wv >> 2;
  u32x4 w2f[43];
  u32 vf = (l & 3) * 128, fl = __builtin_amdgcn_raw_buffer_load_b32(rw, vf, O_FL + g * 512, SC1);
  const u32x4 bq = *(const GLB u32x4*)((const GLB uint16_t*)b2 + 128 * jb + 8 * (t & 15));
  const u32 w2b = O_W2 + ((4 * jb + cg) * 88 + kh * 44) * 1024;
#define W2LD(i) w2f[i] = bld(rw, l * 16 + ((i) & 3) * 1024, w2b + ((i) >> 2) * 4096)
  {
    f32x4 acc[3][2];
#pragma unroll
    for (int c = 2; c < 3; ++c) { acc[c][0] = z4; acc[c][1] = z4; PIN(acc[c][0]); PIN(acc[c][1]); }
    {
      const bool gm = wv >= 6;
      if (gm) __builtin_amdgcn_s_setprio(1);
      if (gm) {
        const u32x4 ones = {0x3F803F80u, 0x3F803F80u, 0x3F803F80u, 0x3F803F80u};
        LDS char* gp = ap + (wv & 1) * 16640;
        u32x4 q[8]; f32x4 s1 = z4, s2 = z4;
#pragma unroll
        for (int i = 0; i < 8; ++i) q[i] = ldsr(gp + i * 16);
#pragma unroll
        for (int i = 0; i < 16; ++i) { s2 = mm(q[i & 7], q[i & 7], s2); s1 = mm(ones, q[i & 7], s1); SB(); if (i < 8) q[i] = ldsr(gp + (i + 8) * 16); SB(); }
        const int e = r & 3;
        float mb, inv; lnstat(s1[0], e == 0 ? s2[0] : e == 1 ? s2[1] : e == 2 ? s2[2] : s2[3], mb, inv);
        if (h == (r >> 2)) *(LDS f32x2*)(sm + L_ST + ((wv - 6) * 16 + r) * 8) = f32x2{mb, inv};
        bar();
      }
      u32x4 a0[2][2], a1[2][2]; u32x2 w3[2][4];
#pragma unroll
      for (int u = 0; u < 2; ++u) { a0[0][u] = ldsr(ap + u * 16); a1[0][u] = ldsr(ap + 16640 + u * 16); }
#pragma unroll
      for (int u = 0; u < 4; ++u) w3[0][u] = ldst(tp + u * 3072);
      SB();
#pragma unroll
      for (int kk = 0; kk < 8; ++kk) {
        const int p = kk & 1;
        if (kk == PRK) __builtin_amdgcn_s_setprio(0);
#pragma unroll
        for (int u = 0; u < 2; ++u)
#pragma unroll
          for (int c = 0; c < 2; ++c) {
            mma(acc[c][0], wf[c][2 * kk + u], a0[p][u], kk + u == 0);
            SB();
            if (kk < 7) { if (u == 0) a0[p ^ 1][c] = ldsr(ap + (2 * kk + 2 + c) * 16); else w3[p ^ 1][2 * c] = ldst(tp + (4 * kk + 4 + 2 * c) * 3072); }
            SB();
            mma(acc[c][1], wf[c][2 * kk + u], a1[p][u], kk + u == 0);
            SB();
            if (kk < 7) { if (u == 0) a1[p ^ 1][c] = ldsr(ap + 16640 + (2 * kk + 2 + c) * 16); else w3[p ^ 1][2 * c + 1] = ldst(tp + (4 * kk + 5 + 2 * c) * 3072); }
#pragma unroll
            for (int i = NW2A * (4 * kk + 2 * u + c) / 32; i < NW2A * (4 * kk + 2 * u + c + 1) / 32; ++i) W2LD(i);
            SB();
          }
        if (!gm) {
#pragma unroll
          for (int u = 0; u < 2; ++u) {
            const u32x4 w = {w3[p][2 * u][0], w3[p][2 * u][1], w3[p][2 * u + 1][0], w3[p][2 * u + 1][1]};
            mma(acc[2][0], w, a0[p][u], false); mma(acc[2][1], w, a1[p][u], false);
          }
          if (kk == 2) asm volatile("s_barrier" ::: "memory");
        }
        SB();
      }
      asm volatile("s_nop 7\n\ts_nop 7" : "+v"(acc[0][0]), "+v"(acc[0][1]), "+v"(acc[1][0]), "+v"(acc[1][1]), "+v"(acc[2][0]), "+v"(acc[2][1]));
    }
    if (!all(fl == tag)) {
#pragma unroll 1
      for (int it = 0; it < SPIN; ++it) { asm volatile("" : "+v"(vf) :: "memory"); if (all(__builtin_amdgcn_raw_buffer_load_b32(rw, vf, O_FL + g * 512, SC1) == tag)) break; }
    }
    const f32x2 st0 = *(LDS f32x2*)(sm + L_ST + r * 8), st1 = *(LDS f32x2*)(sm + L_ST + (16 + r) * 8);
#pragma unroll
    for (int c = 0; c < 3; ++c) {
      if (c < 2 || has3) {
        const int ft = ft0 + 8 * c;
        const f32x4 cs = *(LDS f32x4*)(sm + L_CS + (wv + 8 * c) * 64 + h * 16), bb = *(LDS f32x4*)(sm + L_BB + (wv + 8 * c) * 64 + h * 16);
        float v0[4], v1[4];
#pragma unroll
        for (int j = 0; j < 4; ++j) {
          float p0 = __builtin_fmaxf((acc[c][0][j] - st0[0] * cs[j]) * st0[1] + bb[j], 0.f), p1 = __builtin_fmaxf((acc[c][1][j] - st1[0] * cs[j]) * st1[1] + bb[j], 0.f);
          v0[j] = p0 * p0; v1[j] = p1 * p1;
        }
        auto s0 = __builtin_amdgcn_permlane16_swap(pk(v0[0], v0[1]), pk(v1[0], v1[1]), false, false);
        auto s1 = __builtin_amdgcn_permlane16_swap(pk(v0[2], v0[3]), pk(v1[2], v1[3]), false, false);
        const u32x4 o = {(u32)s0[0], (u32)s1[0], (u32)s0[1], (u32)s1[1]};
        bst(o, rw, l * 16, O_HB + (g * 88 + ft) * 1024);
        ldsw(sm + L_H + (wv + 8 * c) * 1024 + l * 16, o);
      }
    }
  }

  {
    SB();
    if (!kh) {
#pragma unroll
      for (int i = NW2A; i < NW2B; ++i) W2LD(i);
    }
#pragma unroll
    for (int i = 0; i < 10; ++i) PIN(w2f[i]);
    SB();
    const u32 hsrc = O_HB + (g * 88 + wv) * 1024;
    auto hdma = [&]() {
#pragma unroll
      for (int m = 1; m < 4; ++m) {
        const int pj = (jb + m) & 3; const u32 sl = L_H + (22 * (pj - jb) + (pj < jb ? 86 : 0) + wv) * 1024;
        dma(rw, sl, l * 16, hsrc + pj * 22528); dma(rw, sl + 8192, l * 16, hsrc + pj * 22528 + 8192);
        if (wv < (pj == 3 ? 4 : 6)) dma(rw, sl + 16384, l * 16, hsrc + pj * 22528 + 16384);
      }
    };
    LDS char* hb = sm + L_H + kh * 1024 + l * 16;
    const int tok = t >> 4, e = t & 15, d0 = 128 * jb + 8 * e;
    LDS char* hp = sm + L_HT + tok * 1040 + (((d0 >> 2) & 3) * 16 + (d0 >> 5)) * 16 + ((d0 >> 4) & 1) * 8;
    const u32x2 h0 = *(LDS u32x2*)hp, h1 = *(LDS u32x2*)(hp + 256);
    bar();
    if (!kh) hdma();
    f32x16 acc = {0.f};
    float res[8];
    u32x4 hq[4];
#pragma unroll
    for (int i = 0; i < 3; ++i) hq[i] = ldsr(hb + i * 2048);
#pragma unroll
    for (int i = 0; i < 10; ++i) { if (i + 3 < 10) hq[(i + 3) & 3] = ldsr(hb + (i + 3) * 2048); SB(); acc = mm32(w2f[i], hq[i & 3], acc); SB(); }
#pragma unroll
    for (int i = 0; i < 4; ++i) { const u32 hv = i < 2 ? h0[i & 1] : h1[i & 1]; res[2 * i] = blo(bq[i]) + blo(hv); res[2 * i + 1] = bhi(bq[i]) + bhi(hv); PIN(res[2 * i]); PIN(res[2 * i + 1]); }
    LDS char* wp = sm + L_RED + (kh * 32 + (l & 31)) * 528 + (32 * cg + 4 * (l >> 5)) * 4;
    LDS char* rp = sm + L_RED + tok * 528 + (e >> 3) * 256 + (e & 7) * 16;
    u32 vo = ((srow0 + 32 * (tok >> 2) + (tok & 3)) * D_ + d0) * 2;
    LDS char* cp[3];
#pragma unroll
    for (int m = 1; m < 4; ++m) {
      const int pj = (jb + m) & 3;
      cp[m - 1] = sm + L_H + (22 * (pj - jb) + (pj < jb ? 86 : 0) + wv) * 1024 + umin(h, wv < (pj == 3 ? 4 : 6) ? 2 : 1) * 8192 + r * 64;
      PIN(cp[m - 1]);
    }
    PIN(wp); PIN(rp); PIN(vo);
    SB();
    if (kh) {
      WAITV(0);
      hdma();
#pragma unroll
      for (int i = NW2A; i < NW2C; ++i) W2LD(i);
      asm volatile("s_waitcnt vmcnt(%0)" :: "n"(NW2C - NW2A) : "memory");
    } else WAITV(0);
#pragma unroll 1
    for (int it = 0; it < SPIN; ++it) {
      if (all(umax(umax(*(LDS u32*)cp[0], *(LDS u32*)cp[1]), *(LDS u32*)cp[2]) != POISON)) break;
      hdma();
      WAITV(0);
    }
    bar();
#pragma unroll
    for (int i = 10; i < 13; ++i) hq[i & 3] = ldsr(hb + i * 2048);
#pragma unroll
    for (int i = 10; i < 43; ++i) {
      if (i + 3 < 43) hq[(i + 3) & 3] = ldsr(hb + (i + 3) * 2048);
      const int j = NW2C + i - 10;
      if (j < NW2B) { if ((i - 10) % 2 == 0 && kh) { W2LD(j); if (j + 1 < NW2B) W2LD(j + 1); } } else if (j < 43) W2LD(j);
      SB();
      acc = mm32(w2f[i], hq[i & 3], acc);
      SB();
    }
#pragma unroll
    for (int q = 0; q < 4; ++q) *(LDS f32x4*)(wp + 32 * q) = f32x4{acc[4 * q], acc[4 * q + 1], acc[4 * q + 2], acc[4 * q + 3]};
    if (acc[0] != acc[0]) *(LDS u32*)(sm + L_FL) = 1;
    bar();
    f32x4 a0 = *(LDS f32x4*)rp + *(LDS f32x4*)(rp + 32 * 528), a1 = *(LDS f32x4*)(rp + 128) + *(LDS f32x4*)(rp + 32 * 528 + 128);
    if (__builtin_amdgcn_readfirstlane(*(LDS u32*)(sm + L_FL))) {
#pragma unroll 1
      for (int f = wv; f < 86; f += 8) {
        int ft = f + 22 * jb; ft = ft >= 86 ? ft - 86 : ft;
        u32x4 q; u32 vh = l * 16;
#pragma unroll 1
        for (int it = 0; it < SPIN; ++it) {
          asm volatile("" : "+v"(vh) :: "memory");
          q = bld(rw, vh, O_HB + (g * 88 + ft) * 1024);
          if (all(clean(q))) break;
        }
        ldsw(sm + L_H + f * 1024 + l * 16, q);
      }
      bar();
      acc = f32x16{0.f};
#pragma unroll 1
      for (int i = 0; i < 43; ++i) acc = mm32(bld(rw, l * 16, w2b + i * 1024), ldsr(hb + i * 2048), acc);
#pragma unroll
      for (int q = 0; q < 4; ++q) *(LDS f32x4*)(wp + 32 * q) = f32x4{acc[4 * q], acc[4 * q + 1], acc[4 * q + 2], acc[4 * q + 3]};
      bar();
      a0 = *(LDS f32x4*)rp + *(LDS f32x4*)(rp + 32 * 528); a1 = *(LDS f32x4*)(rp + 128) + *(LDS f32x4*)(rp + 32 * 528 + 128);
    }
    u32x4 o;
#pragma unroll
    for (int i = 0; i < 4; ++i) o[i] = pk(a0[i] + res[2 * i], a1[i] + res[2 * i + 1]);
    __builtin_amdgcn_raw_buffer_store_b128(o, mkrs(out, S_ * D_ * 2), vo, 0, SC1);
  }
  asm volatile(".Lpf_b:");
}

#ifdef __HIP_DEVICE_COMPILE__
#pragma clang attribute pop
#endif
extern "C" void kernel_launch(void* const* d_in, const int* in_sizes, int n_in, void* d_out, int out_size, void* d_ws, size_t ws_size, hipStream_t stream) {
  (void)hipFuncSetAttribute((const void*)k_all, hipFuncAttributeMaxDynamicSharedMemorySize, L_END);
  k_all<<<256, 512, L_END, stream>>>((const uint16_t*)d_in[0], (const uint16_t*)d_in[1], (const uint16_t*)d_in[2], (const uint16_t*)d_in[4], (const uint16_t*)d_in[5],
                                     (const uint16_t*)d_in[6], (const uint16_t*)d_in[7], (char*)d_ws, (uint16_t*)d_out);
}
```

```cpp
#include <hip/hip_runtime.h>
#include <hip/hip_bf16.h>
#include <stdint.h>

#ifdef __HIP_DEVICE_COMPILE__
#pragma clang attribute push(__attribute__((target("no-packed-fp32-ops"))), apply_to = function)
#endif
typedef unsigned u32;
typedef u32 u32x2 __attribute__((ext_vector_type(2)));
typedef u32 u32x4 __attribute__((ext_vector_type(4)));
typedef float f32x4 __attribute__((ext_vector_type(4)));
typedef float f32x2 __attribute__((ext_vector_type(2)));
typedef float f32x16 __attribute__((ext_vector_type(16)));
typedef __bf16 bf16x8 __attribute__((ext_vector_type(8)));
typedef __bf16 bf16x2 __attribute__((ext_vector_type(2)));
typedef __amdgpu_buffer_rsrc_t rsrc_t;
#define LDS __attribute__((address_space(3)))
#define GLB __attribute__((address_space(1)))
#define CST __attribute__((address_space(4)))
#define DEV __device__ __forceinline__

constexpr int S_ = 2048, D_ = 512, F_ = 1376, DF_ = 257;
constexpr u32 PST = 8448, PS1 = 12544;
constexpr u32 O_X1 = 0, O_WT = 0x320000, O_X2 = 0x540000, O_W1 = 0x760000, O_W2 = 0x8C0000,
              O_CB = 0xA20000, O_HB = 0xA40000, O_FL = 0xFC0000, O_EP = 0x1000000, WS_END = 0x1001000;
constexpr u32 POISON = 0xFFFFFFFFu;
constexpr int SC1 = 16;
constexpr int SPIN = 1 << 15;
constexpr int NJ2 = 24, NW1A = 8, NW2A = 24, NW2B = 36, NW2C = 28, NDM = 3, PRK = 7;
#define PIN(x) asm volatile("" : "+v"(x))
#define SB() __builtin_amdgcn_sched_barrier(0)
#define WAITV(n) asm volatile("s_waitcnt vmcnt(" #n ")" ::: "memory")

DEV rsrc_t mkrs(const void* p, u32 bytes) { return __builtin_amdgcn_make_buffer_rsrc((void*)p, 0, bytes, 0x00020000); }
DEV u32 pk(float a, float b) { f32x2 v = {a, b}; return __builtin_bit_cast(u32, __builtin_convertvector(v, bf16x2)); }
DEV float blo(u32 v) { return __builtin_bit_cast(float, v << 16); }
DEV float bhi(u32 v) { return __builtin_bit_cast(float, v & 0xFFFF0000u); }
DEV float rbf(float a) { return blo(pk(a, a)); }
DEV float cosr(float rev) { return __builtin_amdgcn_cosf(rev); }
DEV float sinr(float rev) { return __builtin_amdgcn_sinf(rev); }
DEV f32x4 mm(u32x4 a, u32x4 b, f32x4 c) { return __builtin_amdgcn_mfma_f32_16x16x32_bf16(__builtin_bit_cast(bf16x8, a), __builtin_bit_cast(bf16x8, b), c, 0, 0, 0); }
DEV f32x16 mm32(u32x4 a, u32x4 b, f32x16 c) { return __builtin_amdgcn_mfma_f32_32x32x16_bf16(__builtin_bit_cast(bf16x8, a), __builtin_bit_cast(bf16x8, b), c, 0, 0, 0); }
template <int C> DEV float dpp(float x) { return __builtin_bit_cast(float, __builtin_amdgcn_mov_dpp(__builtin_bit_cast(int, x), C, 0xf, 0xf, true)); }
DEV float wsum(float v) {
  v += dpp<0xB1>(v); v += dpp<0x4E>(v); v += dpp<0x141>(v); v += dpp<0x128>(v);
  u32 u = __builtin_bit_cast(u32, v);
  auto a = __builtin_amdgcn_permlane16_swap(u, u, false, false);
  v = __builtin_bit_cast(float, (u32)a[0]) + __builtin_bit_cast(float, (u32)a[1]);
  u = __builtin_bit_cast(u32, v);
  auto b = __builtin_amdgcn_permlane32_swap(u, u, false, false);
  return __builtin_bit_cast(float, (u32)b[0]) + __builtin_bit_cast(float, (u32)b[1]);
}
DEV u32x4 ldsr(LDS char* p) { return *(LDS u32x4*)p; }
DEV void ldsw(LDS char* p, u32x4 v) { *(LDS u32x4*)p = v; }
DEV u32 umax(u32 a, u32 b) { return a > b ? a : b; }
DEV u32 umin(u32 a, u32 b) { return a < b ? a : b; }
DEV bool all(bool p) { return __builtin_amdgcn_ballot_w64(!p) == 0; }
DEV void bar() { asm volatile("s_waitcnt lgkmcnt(0)" ::: "memory"); __builtin_amdgcn_s_barrier(); asm volatile("" ::: "memory"); }
DEV void lnstat(float s1, float s2, float& mb, float& inv) {
  float m = s1 * (1.f / 512.f);
  float v = __builtin_fmaxf(s2 * (1.f / 512.f) - m * m, 0.f);
  mb = rbf(m);
  inv = __builtin_amdgcn_rcpf(rbf(__builtin_amdgcn_sqrtf(rbf(rbf(v) + 1e-5f))));
}

DEV float half_sum(float c) {
  u32 u = __builtin_bit_cast(u32, c);
  auto a = __builtin_amdgcn_permlane16_swap(u, u, false, false);
  c = __builtin_bit_cast(float, (u32)a[0]) + __builtin_bit_cast(float, (u32)a[1]);
  u = __builtin_bit_cast(u32, c);
  auto b = __builtin_amdgcn_permlane32_swap(u, u, false, false);
  return __builtin_bit_cast(float, (u32)b[0]) + __builtin_bit_cast(float, (u32)b[1]);
}
DEV u32x4 bld(rsrc_t w, u32 v, u32 s) { return __builtin_amdgcn_raw_buffer_load_b128(w, v, s, SC1); }
DEV void bst(u32x4 d, rsrc_t w, u32 v, u32 s) { __builtin_amdgcn_raw_buffer_store_b128(d, w, v, s, SC1); asm volatile("s_nop 1" :: "v"(d) : "memory"); }
DEV void dma(rsrc_t w, u32 lds, u32 v, u32 s) { asm volatile("s_mov_b32 m0, %0\n\ts_nop 0\n\tbuffer_load_dwordx4 %1, %2, %3 offen sc1 lds" :: "s"(lds), "v"(v), "s"(w), "s"(s) : "memory"); }
DEV void dmap(rsrc_t w, u32 lds, u32 v, u32 s) { asm volatile("s_mov_b32 m0, %0\n\ts_nop 0\n\tbuffer_load_dwordx4 %1, %2, %3 offen lds" :: "s"(lds), "v"(v), "s"(w), "s"(s) : "memory"); }
DEV void touchp(rsrc_t w, u32 lds, u32 v, u32 s) { asm volatile("s_mov_b32 m0, %0\n\ts_nop 0\n\tbuffer_load_dword %1, %2, %3 offen lds" :: "s"(lds), "v"(v), "s"(w), "s"(s) : "memory"); }
DEV void mma(f32x4& c, u32x4 a, u32x4 b, bool first) {
  if (first) asm volatile("v_mfma_f32_16x16x32_bf16 %0, %1, %2, 0" : "=&v"(c) : "v"(a), "v"(b));
  else asm volatile("v_mfma_f32_16x16x32_bf16 %0, %1, %2, %0" : "+v"(c) : "v"(a), "v"(b));
}
DEV u32x4 ald4(rsrc_t w, u32 v, u32 s) { u32x4 d; asm volatile("buffer_load_dwordx4 %0, %1, %2, %3 offen" : "=v"(d) : "v"(v), "s"(w), "s"(s) : "memory"); return d; }
DEV u32 ald1(rsrc_t w, u32 v, u32 s) { u32 d; asm volatile("buffer_load_dword %0, %1, %2, %3 offen" : "=v"(d) : "v"(v), "s"(w), "s"(s) : "memory"); return d; }
DEV u32x2 ald2(const void* p, u32 v) { u32x2 d; asm volatile("global_load_dwordx2 %0, %1, %2" : "=v"(d) : "v"(v), "s"(p) : "memory"); return d; }
typedef short s16x4 __attribute__((ext_vector_type(4)));
DEV u32x2 ldst(LDS char* p) { return __builtin_bit_cast(u32x2, __builtin_amdgcn_ds_read_tr16_b64_v4i16((LDS s16x4*)p)); }
template <int N> struct IC { static constexpr int v = N; };
DEV bool clean(u32x4 a) { return umax(umax(a[0], a[1]), umax(a[2], a[3])) != POISON; }

constexpr int L_H = 0;
constexpr int L_RED = 90112;
constexpr int L_B0 = 98304, L_B1 = L_B0 + 8192;
constexpr int L_HT = 123904;
constexpr int L_CC = L_HT + 9216 + 8192, L_CA = L_HT + 9216;
constexpr int L_ST = L_HT + 33280;
constexpr int L_CS = L_ST + 256, L_BB = L_CS + 1408;
constexpr int L_FL = L_BB + 1408, L_DUM = L_FL + 256, L_END = L_DUM + 256;

__global__ __launch_bounds__(512, 2) void k_all(const uint16_t* __restrict__ x, const uint16_t* __restrict__ wr, const uint16_t* __restrict__ wi,
                                                const uint16_t* __restrict__ w1, const uint16_t* __restrict__ b1, const uint16_t* __restrict__ w2,
                                                const uint16_t* __restrict__ b2, char* ws, uint16_t* __restrict__ out) {
  extern __shared__ __attribute__((aligned(16))) char smem_[];
  LDS char* sm = (LDS char*)smem_;
  asm volatile("" : "+s"(x), "+s"(wr), "+s"(wi), "+s"(w1), "+s"(b1), "+s"(w2), "+s"(b2), "+s"(ws), "+s"(out));
  const int t = threadIdx.x, l = t & 63, wv = __builtin_amdgcn_readfirstlane(t >> 6), r = l & 15, h = l >> 4;
  const int bid = blockIdx.x;
  const int cb = (bid & 7) * 32 + (bid >> 3), g = cb >> 2, jb = cb & 3;
  const rsrc_t rw = mkrs(ws, WS_END);
  const f32x4 z4 = {0.f, 0.f, 0.f, 0.f};
  const u32x4 P4 = {POISON, POISON, POISON, POISON};
  const int srow0 = 512 * (g >> 4) + 256 * (g & 1) + 16 * ((g >> 3) & 1) + 4 * ((g >> 1) & 3);
  const int ft0 = 22 * jb + wv;
  const bool has3 = wv < 6 && ft0 + 16 < 86;
  const u32 tag = *(const CST u32*)(ws + O_EP) + 1u;
  const int i0q = (t * 21846) >> 16, i0p = t - 3 * i0q, i1q = ((t + 512) * 21846) >> 16, i1p = t + 512 - 3 * i1q;
  u32x4 xf[4]; u32 bv;

  auto w3dma = [&](int g0, int gs, auto n) {
    const rsrc_t r1 = mkrs(w1, D_ * F_ * 2);
    const u32 va = (l >> 3) * (F_ * 2) + ((l ^ (l >> 3 & 6)) & 7) * 16, vb = (l >> 2) * (F_ * 2) + ((l ^ (l >> 3 & 2)) & 3) * 16;
#pragma unroll
    for (int i = 0; i < n.v; ++i) {
      const int G = g0 + gs * i; const u32 so = G * (16 * F_ * 2) + 704 * jb + 512;
      dmap(r1, L_H + G * 3072, va, so); dmap(r1, L_H + G * 3072 + 1024, va, so + 8 * F_ * 2); dmap(r1, L_H + G * 3072 + 2048, vb, so + (jb == 3 ? 0 : 128));
    }
  };
  {
    const rsrc_t rx = mkrs(x, S_ * D_ * 2);
    u32x4 xa = ald4(rx, 64 * r + 16 * h, (256 * (bid >> 5) + 32 * wv + (bid & 31)) * (D_ * 2));
    const int ksr = ((bid & 63) + 64 * (8 * (bid >> 6) + wv)) * DF_;
    u32x2 wa = ald2(wr + ksr, 8 * l), wb = ald2(wi + ksr, 8 * l);
    u32 na = ((const CST u32*)wr)[(ksr >> 1) + 128], nb = ((const CST u32*)wi)[(ksr >> 1) + 128];
    SB();
    u32 v[8];
    const int id = (wv < 3 ? wv : wv - 3) * 256 + bid, tp = id >> 4;
    const bool t1 = wv < 3 && id < 688, t2 = wv >= 3 && id < 688, nat = tp - 11 * ((tp * 47) >> 9) >= 8;
    if (t1) {
      const rsrc_t rt = mkrs(w1, D_ * F_ * 2);
      const u32 so = (id & 15) * (64 * F_) + 64 * tp, vo = h * (8 * F_) + 4 * r;
#pragma unroll
      for (int j = 0; j < 8; ++j) v[j] = ald1(rt, vo, so + ((j >> 2) * 16 + (j & 3)) * (F_ * 2));
    } else if (t2) {
      const rsrc_t rt = mkrs(w2, D_ * F_ * 2);
      const int sh = nat ? 10 : 11;
      const u32 so = tp * 32768 + ((id & 8) << (nat ? 11 : 7)) + 128 * (id & 7), vo = ((l >> 5) << (sh + 3)) + 4 * (l & 31);
#pragma unroll
      for (int j = 0; j < 8; ++j) v[j] = ald1(rt, vo, so + (j << sh));
    } else if (wv == 7) {
      unsigned long long pc; u32 len;
      asm volatile("s_getpc_b64 %0\n.Lpf_a:\n\ts_mov_b32 %1, .Lpf_b-.Lpf_a" : "=s"(pc), "=s"(len));
      const rsrc_t rc = mkrs((const void*)pc, len);
#pragma unroll
      for (int i = 0; i < 3; ++i) touchp(rc, L_DUM, l * 128, i * 8192);
    }
    SB();
    {
      const int i = wv & 3, n2 = 4 * h + i;
      LDS char* cp = sm + L_CA + l * 16 + i * 4;
      *(LDS u32*)(cp + (L_CC - L_CA) + 8192 + (wv >> 2) * 1024) = (r >> 1) + 8 * (wv >> 2) == n2 ? 0x3F80u << (16 * (r & 1)) : 0u;
      if (wv < 4) {
        const float a0 = (float)((2 * n2 * r) & 31) * (1.f / 32.f), a1 = (float)(((2 * n2 + 1) * r) & 31) * (1.f / 32.f);
        *(LDS u32*)cp = pk(cosr(a0), cosr(a1));
        *(LDS u32*)(cp + 1024) = r == 0 ? 0xBF803F80u : pk(-sinr(a0), -sinr(a1));
        const float at = (float)(r * n2) * (1.f / 512.f);
        *(LDS float*)(cp + 2048) = cosr(at); *(LDS float*)(cp + 3072) = sinr(at);
      } else {
        const float ph = (float)((r * n2) & 15) * (1.f / 16.f), c = cosr(ph), sn = sinr(ph);
        *(LDS u32*)(cp + 4096) = pk(c, sn); *(LDS u32*)(cp + 5120) = pk(-sn, c) ^ (r >= 8 ? 0x80008000u : 0u);
        const float ps = (float)((r * n2) & 31) * (1.f / 32.f);
        const u32 cs = pk(cosr(ps), r == 0 ? ((i & 1) ? -1.f : 1.f) : -sinr(ps));
        *(LDS u32*)(cp + 6144) = (r & 1) ? cs << 16 : cs & 0xFFFFu;
        *(LDS u32*)(cp + 7168) = (r & 1) ? cs & 0xFFFF0000u : cs >> 16;
      }
    }
    const int k1 = (t >> 3) & 15, k2 = ((t >> 5) & 12) | ((t >> 1) & 3), ka = k1 + 32 * k2;
    LDS char* kp = sm + L_B1 + (t & 1) * 4160 + (k1 ? (wv < 4 ? ka : 512 - ka) : 16 * k2) * 4;
    PIN(kp);
    SB();
    bst(P4, rw, t * 16, O_X2 + cb * PST);
#pragma unroll
    for (int i = 0; i < 4; ++i) xf[i] = ald4(rx, 64 * r + 16 * h, (srow0 + 32 * wv + i) * (D_ * 2));
    SB();
    asm volatile("s_cmp_eq_u32 %11, 1\n\ts_cbranch_scc1 1f\n\ts_cmp_eq_u32 %11, 2\n\ts_waitcnt vmcnt(8)\n\ts_cbranch_scc1 1f\n\ts_waitcnt vmcnt(5)\n1:\n\ts_waitcnt vmcnt(13)"
                 : "+v"(xa), "+v"(wa), "+v"(wb), "+v"(v[0]), "+v"(v[1]), "+v"(v[2]), "+v"(v[3]), "+v"(v[4]), "+v"(v[5]), "+v"(v[6]), "+v"(v[7])
                 : "s"(__builtin_amdgcn_readfirstlane(t2 ? 1 : wv == 7 ? 2 : 0)) : "scc", "memory");
    asm volatile("" : "+s"(na), "+s"(nb));
    ldsw(sm + L_B1 + wv * 1040 + l * 16, u32x4{__builtin_amdgcn_perm(wb[0], wa[0], 0x05040100u), __builtin_amdgcn_perm(wb[0], wa[0], 0x07060302u),
                                               __builtin_amdgcn_perm(wb[1], wa[1], 0x05040100u), __builtin_amdgcn_perm(wb[1], wa[1], 0x07060302u)});
    *(LDS u32*)(sm + L_B1 + wv * 1040 + 1024) = (ksr & 1) ? (na >> 16) | (nb & 0xFFFF0000u) : (na & 0xFFFFu) | (nb << 16);
    u32x4 p0, p1;
    auto split = [&]() {
#pragma unroll
      for (int i = 0; i < 4; ++i) { p0[i] = __builtin_amdgcn_perm(v[2 * i + 1], v[2 * i], 0x05040100u); p1[i] = __builtin_amdgcn_perm(v[2 * i + 1], v[2 * i], 0x07060302u); }
    };
    if (t1) {
      split();
      const u32 o = O_W1 + ((id >> 4) * 32 + (id & 15)) * 1024;
      if (!nat) { bst(p0, rw, l * 16, o); bst(p1, rw, l * 16, o + 16384); }
      const u32x4 ones = {0x3F803F80u, 0x3F803F80u, 0x3F803F80u, 0x3F803F80u};
      const float c0 = mm(ones, p0, z4)[0], c1 = mm(ones, p1, z4)[0];
      if (l < 32) __builtin_amdgcn_raw_buffer_store_b32(__builtin_bit_cast(u32, l < 16 ? c0 : c1), rw, nat ? 8 * r + 4 * h : l * 4, O_CB + (id & 15) * 5632 + (id >> 4) * 128, SC1);
    }
    bar();
    const f32x4 d0 = mm(xa, ldsr(sm + L_CC + 8192 + l * 16), z4), d1 = mm(xa, ldsr(sm + L_CC + 9216 + l * 16), z4);
    { u32x4 q;
#pragma unroll
      for (int i = 0; i < 4; ++i) q[i] = *(LDS u32*)(kp + i * 1040);
      bst(q, rw, t * 16, O_WT + bid * PST); }
    LDS char* cq = sm + L_CA + l * 16;
    const u32x4 C1 = ldsr(cq), S1 = ldsr(cq + 1024), R2 = ldsr(cq + 4096), I2 = ldsr(cq + 5120), SR = ldsr(cq + 6144), SI = ldsr(cq + 7168);
    const f32x4 tc = *(LDS f32x4*)(cq + 2048), ts = *(LDS f32x4*)(cq + 3072);
    if (wv == 7) {
      u32x4 q;
#pragma unroll
      for (int i = 0; i < 4; ++i) q[i] = *(LDS u32*)(sm + L_B1 + (4 * (t & 1) + i) * 1040 + 1024);
      if (l < 2) bst(q, rw, 8192 + l * 16, O_WT + bid * PST);
    }
    SB();
    float s1 = 0.f, s2 = 0.f;
#pragma unroll
    for (int j = 0; j < 4; ++j) { s1 += d0[j] + d1[j]; s2 = __builtin_fmaf(d0[j], d0[j], __builtin_fmaf(d1[j], d1[j], s2)); }
    s1 = wsum(s1); s2 = wsum(s2);
    float mb, inv; lnstat(s1, s2, mb, inv);
    u32x4 a;
#pragma unroll
    for (int j = 0; j < 4; ++j) a[j] = pk((d0[j] - mb) * inv, (d1[j] - mb) * inv);
    f32x4 yr = mm(a, C1, z4), yi = mm(a, S1, z4);
    u32x4 b;
#pragma unroll
    for (int j = 0; j < 4; ++j) b[j] = pk(yr[j] * tc[j] + yi[j] * ts[j], yi[j] * tc[j] - yr[j] * ts[j]);
    f32x4 xr = mm(R2, b, z4), xi = mm(I2, b, z4), sr = mm(SR, b, z4), si = mm(SI, b, z4);
    u32x4 o;
#pragma unroll
    for (int j = 0; j < 4; ++j) o[j] = pk(r == 0 ? sr[j] : xr[j], r == 0 ? si[j] : xi[j]);
    ldsw(sm + L_HT + wv * 1040 + l * 16, o);
    if (t < 2) *(LDS u32*)(sm + L_FL + t * 4) = 0u;
    WAITV(0);
    PIN(xf[0]); PIN(xf[1]); PIN(xf[2]); PIN(xf[3]);
    bar();
    { u32x4 q; q[3] = tag;
#pragma unroll
      for (int j = 0; j < 3; ++j) q[j] = *(LDS u32*)(sm + L_HT + (3 * i0p + j) * 1040 + i0q * 4);
      bst(q, rw, t * 16, O_X1 + bid * PS1);
      if (wv < 4) {
#pragma unroll
        for (int j = 0; j < 3; ++j) q[j] = *(LDS u32*)(sm + L_HT + (3 * i1p + j) * 1040 + i1q * 4);
        bst(q, rw, t * 16 + 8192, O_X1 + bid * PS1);
      } }
    SB();
    if (t2) {
      int n = tp - 11 * ((id & 7) >> 1); n = n < 0 ? n + 43 : n;
      const u32 o2 = O_W2 + ((id & 7) * 176 + ((id >> 3) & 1) * 44 + n) * 1024;
#pragma unroll
      for (int i = 0; i < 8; ++i) PIN(v[i]);
      split();
      bst(p0, rw, l * 16, o2); bst(p1, rw, l * 16, o2 + 88 * 1024);
    }
    SB();
  }
  if (wv >= 4) {
    w3dma(wv - 4, 4, IC<NDM>());
    const int i = wv & 3, m = 4 * h + i;
    LDS char* cp = sm + L_CC + l * 16 + i * 4;
    float a = (float)((r * m) & 15) * (1.f / 16.f), c = cosr(a), sn = sinr(a);
    const u32 sg = h >= 2 ? 0x80000000u : 0u;
    *(LDS u32*)cp = pk(c, -sn) ^ sg; *(LDS u32*)(cp + 1024) = pk(sn, c) ^ sg;
    *(LDS u32*)(cp + 4096) = m == 0 ? 0x3F803F80u : pk(2.f * c, -2.f * sn);
    a = (float)((r * m) & 31) * (1.f / 32.f);
    const u32 v = m == 0 ? ((r & 1) ? 0xBF803F80u : 0x3F803F80u) : pk(2.f * cosr(a), -2.f * sinr(a));
    *(LDS u32*)(cp + 2048) = (i & 1) ? 0u : v; *(LDS u32*)(cp + 3072) = (i & 1) ? v : 0u;
    a = (float)((m * (2 * r + 1)) & 31) * (1.f / 32.f);
    *(LDS u32*)(cp + 5120) = m == 0 ? 0xBF803F80u : pk(2.f * cosr(a), -2.f * sinr(a));
    a = (float)(r * m) * (1.f / 512.f);
    *(LDS float*)(cp + 6144) = cosr(a); *(LDS float*)(cp + 7168) = sinr(a);
  }
  SB();

  u32x4 wf[2][16];
  const u32 w1b = O_W1 + ft0 * 16384;
#define W1LD(e) wf[(e) & 1][(e) >> 1] = bld(rw, l * 16 + (((e) >> 1) & 3) * 1024, w1b + ((e) & 1) * 131072 + ((e) >> 3) * 4096)
  const int job = wv >> 2;
  {
    const int w = wv & 3;
    const bool act = job == 0 || cb == 0;
    u32x4 wt[2], R1[4], I1[4], R2[2][2], I2[2][2];
    float tc[2][4], ts[2][4], uc[2][4], us[2][4];
    if (act) {
      int lo = l; PIN(lo);
      const int r = lo & 15, h = lo >> 4;
      const int k1 = 16 * w + r;
      const u32 odd = (r & 1) ? 0x80008000u : 0u, two = (r & 2) ? 0x80008000u : 0u;
#pragma unroll
      for (int i = 0; i < 4; ++i) {
        const int m = 4 * h + i;
        float a = (float)((k1 * m) & 63) * (1.f / 64.f), c = cosr(a), sn = sinr(a);
        u32 R = pk(c, sn), I = pk(-sn, c);
        R1[0][i] = R; I1[0][i] = I; R1[2][i] = R ^ odd; I1[2][i] = I ^ odd;
        u32 Rq = ((r & 1) ? I : R) ^ two, Iq = ((r & 1) ? R : I) ^ two ^ odd;
        R1[1][i] = Rq; I1[1][i] = Iq; R1[3][i] = Rq ^ odd; I1[3][i] = Iq ^ odd;
        a = (float)((r * m) & 31) * (1.f / 32.f); c = cosr(a); sn = sinr(a);
        R = pk(c, sn); I = pk(-sn, c);
        const u32 fi = (i & 1) ? 0x80008000u : 0u;
        R2[0][0][i] = R; I2[0][0][i] = I; R2[0][1][i] = R ^ odd; I2[0][1][i] = I ^ odd;
        R2[1][0][i] = R ^ fi; I2[1][0][i] = I ^ fi; R2[1][1][i] = R ^ odd ^ fi; I2[1][1][i] = I ^ odd ^ fi;
#pragma unroll
        for (int u = 0; u < 2; ++u) {
          a = (float)(k1 * (16 * u + m)) * (1.f / 2048.f); tc[u][i] = cosr(a) * 0x1p-20f; ts[u][i] = sinr(a) * 0x1p-20f;
          a = (float)((16 * w + m) * (16 * u + r)) * (1.f / 2048.f); uc[u][i] = cosr(a); us[u][i] = sinr(a);
          PIN(tc[u][i]); PIN(ts[u][i]); PIN(uc[u][i]); PIN(us[u][i]);
        }
      }
#pragma unroll
      for (int i = 0; i < 4; ++i) { PIN(R1[i]); PIN(I1[i]); PIN(R2[i >> 1][i & 1]); PIN(I2[i >> 1][i & 1]); }
    }
    u32 vt = ((h >> 1) * 64 + 16 * w + r) * PST + (h & 1) * 16, vb = umin(t >= 256 ? t : (t & ~31) + 2 * r + (h & 1), jb == 3 ? 319 : 351) * 2;
    PIN(vt); PIN(vb);
    SB();
    WAITV(0);
    {
      const int tt = t - 256, jq = ((tt + 512) * 21846) >> 16, jp = tt + 512 - 3 * jq;
      u32x4 g0, g1; u32 v0 = i0q * PS1 + i0p * 16, v1 = jq * PS1 + jp * 16;
      #pragma unroll 1
    for (int it = 0; it < SPIN; ++it) {
        asm volatile("" : "+v"(v0), "+v"(v1) :: "memory");
        g0 = bld(rw, v0, O_X1 + cb * 48);
        bool ok;
        if (job) { g1 = bld(rw, v1, O_X1 + cb * 48); ok = (g0[3] == tag) & (g1[3] == tag); } else ok = g0[3] == tag;
        if (all(ok)) break;
        __builtin_amdgcn_s_sleep(2);
      }
      LDS char* p = sm + L_B0 + i0q * 32 + i0p * 12;
      *(LDS u32*)p = g0[0]; *(LDS u32*)(p + 4) = g0[1]; if (i0p != 2) *(LDS u32*)(p + 8) = g0[2];
      if (job) { p = sm + L_B0 + jq * 32 + jp * 12; *(LDS u32*)p = g1[0]; *(LDS u32*)(p + 4) = g1[1]; if (jp != 2) *(LDS u32*)(p + 8) = g1[2]; }
    }
    if (cb == 0) {
      bar();
      const u32x4 a = ldsr(sm + L_B0 + t * 16);
      ldsw(sm + L_B0 + t * 16, a & 0xFFFFu); ldsw(sm + L_HT + t * 16, a >> 16);
    }
    bar();
    if (act) {
#pragma unroll
      for (int u = 0; u < 2; ++u) wt[u] = bld(rw, vt + u * 128 * PST, O_WT + (job ? 8192 : cb * 32));
    } else __builtin_amdgcn_s_sleep(1);
    SB();
    bv = __builtin_amdgcn_raw_buffer_load_b16(mkrs(b1, F_ * 2), vb, 704 * jb, 0);
    SB();
    if (act) {
      u32x4 bf[2], af[2][4];
      LDS char* ap = sm + (job ? L_HT : L_B0) + ((h >> 1) * 32 + r) * 32 + (h & 1) * 16;
#pragma unroll
      for (int u = 0; u < 2; ++u)
#pragma unroll
        for (int q = 0; q < 4; ++q) af[u][q] = ldsr(ap + q * 2048 + u * 512);
      SB();
#pragma unroll
      for (int u = 0; u < 2; ++u) {
        f32x4 yr = z4, yi = z4;
#pragma unroll
        for (int q = 0; q < 4; ++q) { yr = mm(af[u][q], R1[q], yr); yi = mm(af[u][q], I1[q], yi); }
#pragma unroll
        for (int j = 0; j < 4; ++j) bf[u][j] = pk(yr[j] * tc[u][j] + yi[j] * ts[u][j], yi[j] * tc[u][j] - yr[j] * ts[u][j]);
      }
      u32x4 ef[2];
#pragma unroll
      for (int u = 0; u < 2; ++u) {
        f32x4 xr = mm(R2[u][1], bf[1], mm(R2[u][0], bf[0], z4)), xi = mm(I2[u][1], bf[1], mm(I2[u][0], bf[0], z4));
#pragma unroll
        for (int j = 0; j < 4; ++j) { float a = blo(wt[u][j]), b = bhi(wt[u][j]); ef[u][j] = pk(xr[j] * a - xi[j] * b, -(xr[j] * b + xi[j] * a)); }
      }
#pragma unroll
      for (int u = 0; u < 2; ++u) {
        f32x4 yr = mm(ef[1], R2[u][1], mm(ef[0], R2[u][0], z4)), yi = mm(ef[1], I2[u][1], mm(ef[0], I2[u][0], z4));
        u32x4 o;
#pragma unroll
        for (int j = 0; j < 4; ++j) o[j] = pk(yr[j] * uc[u][j] + yi[j] * us[u][j], yi[j] * uc[u][j] - yr[j] * us[u][j]);
        ldsw(sm + L_B1 + job * 8192 + ((u * 4 + w) * 64 + l) * 16, o);
      }
      if (l == 0) __hip_atomic_fetch_add((LDS u32*)(sm + L_FL + 4), 1u, __ATOMIC_RELAXED, __HIP_MEMORY_SCOPE_WORKGROUP);
    }
    if (job) {
#pragma unroll
      for (int c = 0; c < 6; ++c) if (w + 4 * c < 22) bst(P4, rw, l * 16, O_HB + (g * 88 + 22 * jb + w + 4 * c) * 1024);
      w3dma(4 * NDM + w, 4, IC<8 - NDM>());
      const int tt = t - 256;
      u32x4 cp[8];
      if (w < 3) {
#pragma unroll
        for (int i = 0; i < 8; ++i) cp[i] = bld(rw, ((tt & 1) * 8 + i) * 5632 + (tt >> 1) * 16, O_CB + jb * 1408);
      }
      SB();
#pragma unroll
      for (int e = 0; e < NJ2; ++e) W1LD(e);
      SB();
      if (w < 3) {
        f32x4 cs = z4;
#pragma unroll
        for (int i = 0; i < 8; ++i) cs += __builtin_bit_cast(f32x4, cp[i]);
#pragma unroll
        for (int j = 0; j < 4; ++j) cs[j] += dpp<0xB1>(cs[j]);
        if (tt < 176) *(LDS f32x4*)(sm + L_CS + (tt >> 1) * 16) = cs;
      }
    } else {
#pragma unroll 1
      for (int it = 0; it < SPIN; ++it) {
        asm volatile("" ::: "memory");
        if (__builtin_amdgcn_readfirstlane(*(volatile LDS u32*)(sm + L_FL + 4)) == (cb ? 4 : 8)) break;
      }
      asm volatile("" ::: "memory");
      u32x4 af[2][4];
#pragma unroll
      for (int u = 0; u < 2; ++u)
#pragma unroll
        for (int q = 0; q < 4; ++q) af[u][q] = ldsr(sm + L_B1 + ((u * 4 + q) * 64 + l) * 16);
      if (cb != 0) {
#pragma unroll
        for (int u = 0; u < 2; ++u) {
          f32x4 zr = z4, zi = z4;
#pragma unroll
          for (int q = 0; q < 4; ++q) { zr = mm(af[u][q], R1[q], zr); zi = mm(af[u][q], I1[q], zi); }
          u32x4 o;
#pragma unroll
          for (int j = 0; j < 4; ++j) o[j] = umin(pk(zr[j], -zi[j]), POISON - 1);
          bst(o, rw, ((w * 2 + u) * 64 + l) * 16, O_X2 + cb * PST);
        }
      } else {
#pragma unroll
        for (int u = 0; u < 2; ++u) {
          f32x4 zr = z4, zi = z4;
#pragma unroll
          for (int q = 0; q < 4; ++q) { zr = mm(af[u][q], R1[q], zr); zi = mm(ldsr(sm + L_B1 + 8192 + ((u * 4 + q) * 64 + l) * 16), R1[q], zi); }
          u32x4 o;
#pragma unroll
          for (int j = 0; j < 4; ++j) o[j] = umin(pk(zr[j], zi[j]), POISON - 1);
          bst(o, rw, ((w * 2 + u) * 64 + l) * 16, O_X2);
        }
        if (t == 0) __builtin_amdgcn_raw_buffer_store_b32(tag, rw, 0, O_EP, SC1);
      }
      SB();
#pragma unroll
      for (int e = 0; e < NW1A; ++e) W1LD(e);
    }
    SB();
  }

  LDS char* ap, *tp;
  {
    LDS char* cq = sm + L_CC + l * 16;
    const u32x4 E0 = ldsr(cq + 8192), E1 = ldsr(cq + 9216);
    u32x4 R = ldsr(cq), I = ldsr(cq + 1024), SA = ldsr(cq + 2048), SB_ = ldsr(cq + 3072), M0 = ldsr(cq + 4096), M1 = ldsr(cq + 5120);
    f32x4 tc = *(LDS f32x4*)(cq + 6144), ts = *(LDS f32x4*)(cq + 7168);
    if (t < 352) *(LDS u32*)(sm + L_BB + t * 4) = bv << 16;
    f32x4 y0[4], y1[4];
#pragma unroll
    for (int i = 0; i < 4; ++i) { y0[i] = mm(E0, xf[i], z4); y1[i] = mm(E1, xf[i], z4); PIN(y0[i]); PIN(y1[i]); }
    PIN(tc); PIN(ts);
    ap = sm + L_HT + r * 1040 + h * 256;
    tp = sm + L_H + (job ? 2048 + (l >> 2) * 64 + ((wv ^ h) & 1) * 32 : (l >> 2) * 128 + ((wv ^ (l >> 3)) & 3) * 32) + (l & 3) * 8;
    PIN(ap); PIN(tp);
    PIN(R); PIN(I); PIN(SA); PIN(SB_); PIN(M0); PIN(M1);
    SB();
    WAITV(0);
    bar();
    if (t == 0) __builtin_amdgcn_raw_buffer_store_b32(tag, rw, 0, O_FL + cb * 128, SC1);
    u32x4 zin[4]; u32 vz = (t >> 3) * PST + (t & 7) * 16;
    #pragma unroll 1
    for (int it = 0; it < SPIN; ++it) {
      asm volatile("" : "+v"(vz) :: "memory");
#pragma unroll
      for (int i = 0; i < 4; ++i) zin[i] = bld(rw, vz, O_X2 + i * 64 * PST + g * 128);
      u32x4 m;
#pragma unroll
      for (int j = 0; j < 4; ++j) m[j] = umax(umax(umax(zin[j][0], zin[j][1]), zin[j][2]), zin[j][3]);
      if (all(clean(m))) break;
      __builtin_amdgcn_s_sleep(2);
    }
#pragma unroll
    for (int i = 0; i < 4; ++i) ldsw(sm + L_HT + (t & 7) * 4160 + ((t >> 3) & 3) * 1024 + ((16 * i + (t >> 5) + ((t & 7) >> 1)) & 63) * 16, zin[i]);
    bar();
    u32x4 q[4], zs;
#pragma unroll
    for (int j = 0; j < 4; ++j) {
      q[j] = ldsr(sm + L_HT + wv * 4160 + j * 1024 + ((l + (wv >> 1)) & 63) * 16);
      zs[j] = *(LDS u32*)(sm + L_HT + wv * 4160 + j * 1024 + (16 * h + (wv >> 1)) * 16 + (r & 3) * 4);
    }
    {
      f32x4 ur[4], ui[4];
      const f32x4 sa = mm(zs, SA, z4), sb = mm(zs, SB_, z4);
      { const u32x4 z = {q[0][0], q[1][0], q[2][0], q[3][0]}; ur[0] = mm(z, R, z4); ui[0] = mm(z, I, z4); }
#pragma unroll
      for (int i = 0; i < 4; ++i) {
        if (i < 3) { const u32x4 z = {q[0][i + 1], q[1][i + 1], q[2][i + 1], q[3][i + 1]}; ur[i + 1] = mm(z, R, z4); ui[i + 1] = mm(z, I, z4); }
        SB();
        if (!job) {
#pragma unroll
          for (int e = NW1A + (NJ2 - NW1A) * i / 4; e < NW1A + (NJ2 - NW1A) * (i + 1) / 4; ++e) W1LD(e);
        }
#pragma unroll
        for (int e = NJ2 + (32 - NJ2) * i / 4; e < NJ2 + (32 - NJ2) * (i + 1) / 4; ++e) W1LD(e);
        ur[i][0] = h == 0 ? sa[i] : ur[i][0]; ui[i][0] = h == 0 ? sb[i] : ui[i][0];
        u32x4 v;
#pragma unroll
        for (int j = 0; j < 4; ++j) v[j] = pk(ur[i][j] * tc[j] - ui[i][j] * ts[j], ui[i][j] * tc[j] + ur[i][j] * ts[j]);
        y0[i] = mm(v, M0, y0[i]); y1[i] = mm(v, M1, y1[i]);
        if (i) ldsw(sm + L_HT + (4 * wv + i - 1) * 1040 + l * 16, u32x4{pk(y0[i - 1][0], y0[i - 1][1]), pk(y0[i - 1][2], y0[i - 1][3]), pk(y1[i - 1][0], y1[i - 1][1]), pk(y1[i - 1][2], y1[i - 1][3])});
      }
      ldsw(sm + L_HT + (4 * wv + 3) * 1040 + l * 16, u32x4{pk(y0[3][0], y0[3][1]), pk(y0[3][2], y0[3][3]), pk(y1[3][0], y1[3][1]), pk(y1[3][2], y1[3][3])});
    }
    bar();
  }

  const int cg = wv & 3, kh = wv >> 2;
  u32x4 w2f[43];
  u32 vf = (l & 3) * 128, fl = __builtin_amdgcn_raw_buffer_load_b32(rw, vf, O_FL + g * 512, SC1);
  const u32x4 bq = *(const GLB u32x4*)((const GLB uint16_t*)b2 + 128 * jb + 8 * (t & 15));
  const u32 w2b = O_W2 + ((4 * jb + cg) * 88 + kh * 44) * 1024;
#define W2LD(i) w2f[i] = bld(rw, l * 16 + ((i) & 3) * 1024, w2b + ((i) >> 2) * 4096)
  {
    f32x4 acc[3][2];
#pragma unroll
    for (int c = 2; c < 3; ++c) { acc[c][0] = z4; acc[c][1] = z4; PIN(acc[c][0]); PIN(acc[c][1]); }
    {
      const bool gm = wv >= 6;
      if (gm) __builtin_amdgcn_s_setprio(1);
      if (gm) {
        const u32x4 ones = {0x3F803F80u, 0x3F803F80u, 0x3F803F80u, 0x3F803F80u};
        LDS char* gp = ap + (wv & 1) * 16640;
        u32x4 q[8]; f32x4 s1 = z4, s2 = z4;
#pragma unroll
        for (int i = 0; i < 8; ++i) q[i] = ldsr(gp + i * 16);
#pragma unroll
        for (int i = 0; i < 16; ++i) { s2 = mm(q[i & 7], q[i & 7], s2); s1 = mm(ones, q[i & 7], s1); SB(); if (i < 8) q[i] = ldsr(gp + (i + 8) * 16); SB(); }
        const int e = r & 3;
        float mb, inv; lnstat(s1[0], e == 0 ? s2[0] : e == 1 ? s2[1] : e == 2 ? s2[2] : s2[3], mb, inv);
        if (h == (r >> 2)) *(LDS f32x2*)(sm + L_ST + ((wv - 6) * 16 + r) * 8) = f32x2{mb, inv};
        bar();
      }
      u32x4 a0[2][2], a1[2][2]; u32x2 w3[2][4];
#pragma unroll
      for (int u = 0; u < 2; ++u) { a0[0][u] = ldsr(ap + u * 16); a1[0][u] = ldsr(ap + 16640 + u * 16); }
#pragma unroll
      for (int u = 0; u < 4; ++u) w3[0][u] = ldst(tp + u * 3072);
      SB();
#pragma unroll
      for (int kk = 0; kk < 8; ++kk) {
        const int p = kk & 1;
        if (kk == PRK) __builtin_amdgcn_s_setprio(0);
#pragma unroll
        for (int u = 0; u < 2; ++u)
#pragma unroll
          for (int c = 0; c < 2; ++c) {
            mma(acc[c][0], wf[c][2 * kk + u], a0[p][u], kk + u == 0);
            SB();
            if (kk < 7) { if (u == 0) a0[p ^ 1][c] = ldsr(ap + (2 * kk + 2 + c) * 16); else w3[p ^ 1][2 * c] = ldst(tp + (4 * kk + 4 + 2 * c) * 3072); }
            SB();
            mma(acc[c][1], wf[c][2 * kk + u], a1[p][u], kk + u == 0);
            SB();
            if (kk < 7) { if (u == 0) a1[p ^ 1][c] = ldsr(ap + 16640 + (2 * kk + 2 + c) * 16); else w3[p ^ 1][2 * c + 1] = ldst(tp + (4 * kk + 5 + 2 * c) * 3072); }
#pragma unroll
            for (int i = NW2A * (4 * kk + 2 * u + c) / 32; i < NW2A * (4 * kk + 2 * u + c + 1) / 32; ++i) W2LD(i);
            SB();
          }
        if (!gm) {
#pragma unroll
          for (int u = 0; u < 2; ++u) {
            const u32x4 w = {w3[p][2 * u][0], w3[p][2 * u][1], w3[p][2 * u + 1][0], w3[p][2 * u + 1][1]};
            mma(acc[2][0], w, a0[p][u], false); mma(acc[2][1], w, a1[p][u], false);
          }
          if (kk == 2) asm volatile("s_barrier" ::: "memory");
        }
        SB();
      }
      asm volatile("s_nop 7\n\ts_nop 7" : "+v"(acc[0][0]), "+v"(acc[0][1]), "+v"(acc[1][0]), "+v"(acc[1][1]), "+v"(acc[2][0]), "+v"(acc[2][1]));
    }
    if (!all(fl == tag)) {
#pragma unroll 1
      for (int it = 0; it < SPIN; ++it) { asm volatile("" : "+v"(vf) :: "memory"); if (all(__builtin_amdgcn_raw_buffer_load_b32(rw, vf, O_FL + g * 512, SC1) == tag)) break; }
    }
    const f32x2 st0 = *(LDS f32x2*)(sm + L_ST + r * 8), st1 = *(LDS f32x2*)(sm + L_ST + (16 + r) * 8);
#pragma unroll
    for (int c = 0; c < 3; ++c) {
      if (c < 2 || has3) {
        const int ft = ft0 + 8 * c;
        const f32x4 cs = *(LDS f32x4*)(sm + L_CS + (wv + 8 * c) * 64 + h * 16), bb = *(LDS f32x4*)(sm + L_BB + (wv + 8 * c) * 64 + h * 16);
        float v0[4], v1[4];
#pragma unroll
        for (int j = 0; j < 4; ++j) {
          float p0 = __builtin_fmaxf((acc[c][0][j] - st0[0] * cs[j]) * st0[1] + bb[j], 0.f), p1 = __builtin_fmaxf((acc[c][1][j] - st1[0] * cs[j]) * st1[1] + bb[j], 0.f);
          v0[j] = p0 * p0; v1[j] = p1 * p1;
        }
        auto s0 = __builtin_amdgcn_permlane16_swap(pk(v0[0], v0[1]), pk(v1[0], v1[1]), false, false);
        auto s1 = __builtin_amdgcn_permlane16_swap(pk(v0[2], v0[3]), pk(v1[2], v1[3]), false, false);
        const u32x4 o = {(u32)s0[0], (u32)s1[0], (u32)s0[1], (u32)s1[1]};
        bst(o, rw, l * 16, O_HB + (g * 88 + ft) * 1024);
        ldsw(sm + L_H + (wv + 8 * c) * 1024 + l * 16, o);
      }
    }
  }

  {
    SB();
    if (!kh) {
#pragma unroll
      for (int i = NW2A; i < NW2B; ++i) W2LD(i);
    }
#pragma unroll
    for (int i = 0; i < 10; ++i) PIN(w2f[i]);
    SB();
    const u32 hsrc = O_HB + (g * 88 + wv) * 1024;
    auto hdma = [&]() {
#pragma unroll
      for (int m = 1; m < 4; ++m) {
        const int pj = (jb + m) & 3; const u32 sl = L_H + (22 * (pj - jb) + (pj < jb ? 86 : 0) + wv) * 1024;
        dma(rw, sl, l * 16, hsrc + pj * 22528); dma(rw, sl + 8192, l * 16, hsrc + pj * 22528 + 8192);
        if (wv < (pj == 3 ? 4 : 6)) dma(rw, sl + 16384, l * 16, hsrc + pj * 22528 + 16384);
      }
    };
    LDS char* hb = sm + L_H + kh * 1024 + l * 16;
    const int tok = t >> 4, e = t & 15, d0 = 128 * jb + 8 * e;
    LDS char* hp = sm + L_HT + tok * 1040 + (((d0 >> 2) & 3) * 16 + (d0 >> 5)) * 16 + ((d0 >> 4) & 1) * 8;
    const u32x2 h0 = *(LDS u32x2*)hp, h1 = *(LDS u32x2*)(hp + 256);
    bar();
    if (!kh) hdma();
    f32x16 acc = {0.f};
    float res[8];
    u32x4 hq[4];
#pragma unroll
    for (int i = 0; i < 3; ++i) hq[i] = ldsr(hb + i * 2048);
#pragma unroll
    for (int i = 0; i < 10; ++i) { if (i + 3 < 10) hq[(i + 3) & 3] = ldsr(hb + (i + 3) * 2048); SB(); acc = mm32(w2f[i], hq[i & 3], acc); SB(); }
#pragma unroll
    for (int i = 0; i < 4; ++i) { const u32 hv = i < 2 ? h0[i & 1] : h1[i & 1]; res[2 * i] = blo(bq[i]) + blo(hv); res[2 * i + 1] = bhi(bq[i]) + bhi(hv); PIN(res[2 * i]); PIN(res[2 * i + 1]); }
    LDS char* wp = sm + L_RED + (kh * 32 + (l & 31)) * 528 + (32 * cg + 4 * (l >> 5)) * 4;
    LDS char* rp = sm + L_RED + tok * 528 + (e >> 3) * 256 + (e & 7) * 16;
    u32 vo = ((srow0 + 32 * (tok >> 2) + (tok & 3)) * D_ + d0) * 2;
    LDS char* cp[3];
#pragma unroll
    for (int m = 1; m < 4; ++m) {
      const int pj = (jb + m) & 3;
      cp[m - 1] = sm + L_H + (22 * (pj - jb) + (pj < jb ? 86 : 0) + wv) * 1024 + umin(h, wv < (pj == 3 ? 4 : 6) ? 2 : 1) * 8192 + r * 64;
      PIN(cp[m - 1]);
    }
    PIN(wp); PIN(rp); PIN(vo);
    SB();
    if (kh) {
      WAITV(0);
      hdma();
#pragma unroll
      for (int i = NW2A; i < NW2C; ++i) W2LD(i);
      asm volatile("s_waitcnt vmcnt(%0)" :: "n"(NW2C - NW2A) : "memory");
    } else WAITV(0);
#pragma unroll 1
    for (int it = 0; it < SPIN; ++it) {
      if (all(umax(umax(*(LDS u32*)cp[0], *(LDS u32*)cp[1]), *(LDS u32*)cp[2]) != POISON)) break;
      hdma();
      WAITV(0);
    }
    bar();
#pragma unroll
    for (int i = 10; i < 13; ++i) hq[i & 3] = ldsr(hb + i * 2048);
#pragma unroll
    for (int i = 10; i < 43; ++i) {
      if (i + 3 < 43) hq[(i + 3) & 3] = ldsr(hb + (i + 3) * 2048);
      const int j = NW2C + i - 10;
      if (j < NW2B) { if ((i - 10) % 2 == 0 && kh) { W2LD(j); if (j + 1 < NW2B) W2LD(j + 1); } } else if (j < 43) W2LD(j);
      SB();
      acc = mm32(w2f[i], hq[i & 3], acc);
      SB();
    }
#pragma unroll
    for (int q = 0; q < 4; ++q) *(LDS f32x4*)(wp + 32 * q) = f32x4{acc[4 * q], acc[4 * q + 1], acc[4 * q + 2], acc[4 * q + 3]};
    if (acc[0] != acc[0]) *(LDS u32*)(sm + L_FL) = 1;
    bar();
    f32x4 a0 = *(LDS f32x4*)rp + *(LDS f32x4*)(rp + 32 * 528), a1 = *(LDS f32x4*)(rp + 128) + *(LDS f32x4*)(rp + 32 * 528 + 128);
    if (__builtin_amdgcn_readfirstlane(*(LDS u32*)(sm + L_FL))) {
#pragma unroll 1
      for (int f = wv; f < 86; f += 8) {
        int ft = f + 22 * jb; ft = ft >= 86 ? ft - 86 : ft;
        u32x4 q; u32 vh = l * 16;
#pragma unroll 1
        for (int it = 0; it < SPIN; ++it) {
          asm volatile("" : "+v"(vh) :: "memory");
          q = bld(rw, vh, O_HB + (g * 88 + ft) * 1024);
          if (all(clean(q))) break;
        }
        ldsw(sm + L_H + f * 1024 + l * 16, q);
      }
      bar();
      acc = f32x16{0.f};
#pragma unroll 1
      for (int i = 0; i < 43; ++i) acc = mm32(bld(rw, l * 16, w2b + i * 1024), ldsr(hb + i * 2048), acc);
#pragma unroll
      for (int q = 0; q < 4; ++q) *(LDS f32x4*)(wp + 32 * q) = f32x4{acc[4 * q], acc[4 * q + 1], acc[4 * q + 2], acc[4 * q + 3]};
      bar();
      a0 = *(LDS f32x4*)rp + *(LDS f32x4*)(rp + 32 * 528); a1 = *(LDS f32x4*)(rp + 128) + *(LDS f32x4*)(rp + 32 * 528 + 128);
    }
    u32x4 o;
#pragma unroll
    for (int i = 0; i < 4; ++i) o[i] = pk(a0[i] + res[2 * i], a1[i] + res[2 * i + 1]);
    __builtin_amdgcn_raw_buffer_store_b128(o, mkrs(out, S_ * D_ * 2), vo, 0, SC1);
  }
  asm volatile(".Lpf_b:");
}

#ifdef __HIP_DEVICE_COMPILE__
#pragma clang attribute pop
#endif
extern "C" void kernel_launch(void* const* d_in, const int* in_sizes, int n_in, void* d_out, int out_size, void* d_ws, size_t ws_size, hipStream_t stream) {
  (void)hipFuncSetAttribute((const void*)k_all, hipFuncAttributeMaxDynamicSharedMemorySize, L_END);
  k_all<<<256, 512, L_END, stream>>>((const uint16_t*)d_in[0], (const uint16_t*)d_in[1], (const uint16_t*)d_in[2], (const uint16_t*)d_in[4], (const uint16_t*)d_in[5],
                                     (const uint16_t*)d_in[6], (const uint16_t*)d_in[7], (char*)d_ws, (uint16_t*)d_out);
}
```

```cpp
#include <hip/hip_runtime.h>
#include <hip/hip_bf16.h>
#include <stdint.h>

#ifdef __HIP_DEVICE_COMPILE__
#pragma clang attribute push(__attribute__((target("no-packed-fp32-ops"))), apply_to = function)
#endif
typedef unsigned u32;
typedef u32 u32x2 __attribute__((ext_vector_type(2)));
typedef u32 u32x4 __attribute__((ext_vector_type(4)));
typedef float f32x4 __attribute__((ext_vector_type(4)));
typedef float f32x2 __attribute__((ext_vector_type(2)));
typedef float f32x16 __attribute__((ext_vector_type(16)));
typedef __bf16 bf16x8 __attribute__((ext_vector_type(8)));
typedef __bf16 bf16x2 __attribute__((ext_vector_type(2)));
typedef __amdgpu_buffer_rsrc_t rsrc_t;
#define LDS __attribute__((address_space(3)))
#define GLB __attribute__((address_space(1)))
#define CST __attribute__((address_space(4)))
#define DEV __device__ __forceinline__

constexpr int S_ = 2048, D_ = 512, F_ = 1376, DF_ = 257;
constexpr u32 PST = 8448, PS1 = 12544;
constexpr u32 O_X1 = 0, O_WT = 0x320000, O_X2 = 0x540000, O_W1 = 0x760000, O_W2 = 0x8C0000,
              O_CB = 0xA20000, O_HB = 0xA40000, O_FL = 0xFC0000, O_EP = 0x1000000, WS_END = 0x1001000;
constexpr u32 POISON = 0xFFFFFFFFu;
constexpr int SC1 = 16;
constexpr int SPIN = 1 << 15;
constexpr int NJ2 = 24, NW1A = 4, NW2A = 24, NW2B = 36, NW2C = 28, NDM = 5, PRK = 7, PZ = 0, XP = 4, WP = 1, NDA = 0, WQ = 1, PB = 1, NPA = 6, PM = 1, NJ1 = 12, NW2D = 36, SLP = 0;
#define PIN(x) asm volatile("" : "+v"(x))
#define SB() __builtin_amdgcn_sched_barrier(0)
#define WAITV(n) asm volatile("s_waitcnt vmcnt(" #n ")" ::: "memory")

DEV rsrc_t mkrs(const void* p, u32 bytes) { return __builtin_amdgcn_make_buffer_rsrc((void*)p, 0, bytes, 0x00020000); }
DEV u32 pk(float a, float b) { f32x2 v = {a, b}; return __builtin_bit_cast(u32, __builtin_convertvector(v, bf16x2)); }
DEV float blo(u32 v) { return __builtin_bit_cast(float, v << 16); }
DEV float bhi(u32 v) { return __builtin_bit_cast(float, v & 0xFFFF0000u); }
DEV float rbf(float a) { return blo(pk(a, a)); }
DEV float cosr(float rev) { return __builtin_amdgcn_cosf(rev); }
DEV float sinr(float rev) { return __builtin_amdgcn_sinf(rev); }
DEV f32x4 mm(u32x4 a, u32x4 b, f32x4 c) { return __builtin_amdgcn_mfma_f32_16x16x32_bf16(__builtin_bit_cast(bf16x8, a), __builtin_bit_cast(bf16x8, b), c, 0, 0, 0); }
DEV f32x16 mm32(u32x4 a, u32x4 b, f32x16 c) { return __builtin_amdgcn_mfma_f32_32x32x16_bf16(__builtin_bit_cast(bf16x8, a), __builtin_bit_cast(bf16x8, b), c, 0, 0, 0); }
template <int C> DEV float dpp(float x) { return __builtin_bit_cast(float, __builtin_amdgcn_mov_dpp(__builtin_bit_cast(int, x), C, 0xf, 0xf, true)); }
DEV float wsum(float v) {
  v += dpp<0xB1>(v); v += dpp<0x4E>(v); v += dpp<0x141>(v); v += dpp<0x128>(v);
  u32 u = __builtin_bit_cast(u32, v);
  auto a = __builtin_amdgcn_permlane16_swap(u, u, false, false);
  v = __builtin_bit_cast(float, (u32)a[0]) + __builtin_bit_cast(float, (u32)a[1]);
  u = __builtin_bit_cast(u32, v);
  auto b = __builtin_amdgcn_permlane32_swap(u, u, false, false);
  return __builtin_bit_cast(float, (u32)b[0]) + __builtin_bit_cast(float, (u32)b[1]);
}
DEV u32x4 ldsr(LDS char* p) { return *(LDS u32x4*)p; }
DEV void ldsw(LDS char* p, u32x4 v) { *(LDS u32x4*)p = v; }
DEV u32 umax(u32 a, u32 b) { return a > b ? a : b; }
DEV u32 umin(u32 a, u32 b) { return a < b ? a : b; }
DEV bool all(bool p) { return __builtin_amdgcn_ballot_w64(!p) == 0; }
DEV void bar() { asm volatile("s_waitcnt lgkmcnt(0)" ::: "memory"); __builtin_amdgcn_s_barrier(); asm volatile("" ::: "memory"); }
DEV void lnstat(float s1, float s2, float& mb, float& inv) {
  float m = s1 * (1.f / 512.f);
  float v = __builtin_fmaxf(s2 * (1.f / 512.f) - m * m, 0.f);
  mb = rbf(m);
  inv = __builtin_amdgcn_rcpf(rbf(__builtin_amdgcn_sqrtf(rbf(rbf(v) + 1e-5f))));
}

DEV float half_sum(float c) {
  u32 u = __builtin_bit_cast(u32, c);
  auto a = __builtin_amdgcn_permlane16_swap(u, u, false, false);
  c = __builtin_bit_cast(float, (u32)a[0]) + __builtin_bit_cast(float, (u32)a[1]);
  u = __builtin_bit_cast(u32, c);
  auto b = __builtin_amdgcn_permlane32_swap(u, u, false, false);
  return __builtin_bit_cast(float, (u32)b[0]) + __builtin_bit_cast(float, (u32)b[1]);
}
DEV u32x4 bld(rsrc_t w, u32 v, u32 s) { return __builtin_amdgcn_raw_buffer_load_b128(w, v, s, SC1); }
DEV void bst(u32x4 d, rsrc_t w, u32 v, u32 s) { __builtin_amdgcn_raw_buffer_store_b128(d, w, v, s, SC1); asm volatile("s_nop 1" :: "v"(d) : "memory"); }
DEV void dma(rsrc_t w, u32 lds, u32 v, u32 s) { asm volatile("s_mov_b32 m0, %0\n\ts_nop 0\n\tbuffer_load_dwordx4 %1, %2, %3 offen sc1 lds" :: "s"(lds), "v"(v), "s"(w), "s"(s) : "memory"); }
DEV void dmap(rsrc_t w, u32 lds, u32 v, u32 s) { asm volatile("s_mov_b32 m0, %0\n\ts_nop 0\n\tbuffer_load_dwordx4 %1, %2, %3 offen lds" :: "s"(lds), "v"(v), "s"(w), "s"(s) : "memory"); }
DEV void touchp(rsrc_t w, u32 lds, u32 v, u32 s) { asm volatile("s_mov_b32 m0, %0\n\ts_nop 0\n\tbuffer_load_dword %1, %2, %3 offen lds" :: "s"(lds), "v"(v), "s"(w), "s"(s) : "memory"); }
DEV void mma(f32x4& c, u32x4 a, u32x4 b, bool first) {
  if (first) asm volatile("v_mfma_f32_16x16x32_bf16 %0, %1, %2, 0" : "=&v"(c) : "v"(a), "v"(b));
  else asm volatile("v_mfma_f32_16x16x32_bf16 %0, %1, %2, %0" : "+v"(c) : "v"(a), "v"(b));
}
DEV u32x4 ald4(rsrc_t w, u32 v, u32 s) { u32x4 d; asm volatile("buffer_load_dwordx4 %0, %1, %2, %3 offen" : "=v"(d) : "v"(v), "s"(w), "s"(s) : "memory"); return d; }
DEV u32 ald1(rsrc_t w, u32 v, u32 s) { u32 d; asm volatile("buffer_load_dword %0, %1, %2, %3 offen" : "=v"(d) : "v"(v), "s"(w), "s"(s) : "memory"); return d; }
DEV u32x2 ald2(const void* p, u32 v) { u32x2 d; asm volatile("global_load_dwordx2 %0, %1, %2" : "=v"(d) : "v"(v), "s"(p) : "memory"); return d; }
typedef short s16x4 __attribute__((ext_vector_type(4)));
DEV u32x2 ldst(LDS char* p) { return __builtin_bit_cast(u32x2, __builtin_amdgcn_ds_read_tr16_b64_v4i16((LDS s16x4*)p)); }
template <int N> struct IC { static constexpr int v = N; };
DEV bool clean(u32x4 a) { return umax(umax(a[0], a[1]), umax(a[2], a[3])) != POISON; }

constexpr int L_H = 0;
constexpr int L_RED = 90112;
constexpr int L_B0 = 98304, L_B1 = L_B0 + 8192;
constexpr int L_HT = 123904;
constexpr int L_CC = L_HT + 9216 + 8192, L_CA = L_HT + 9216;
constexpr int L_ST = L_HT + 33280;
constexpr int L_CS = L_ST + 256, L_BB = L_CS + 1408;
constexpr int L_FL = L_BB + 1408, L_DUM = L_FL + 256, L_END = L_DUM + 256;

__global__ __launch_bounds__(512, 2) void k_all(const uint16_t* __restrict__ x, const uint16_t* __restrict__ wr, const uint16_t* __restrict__ wi,
                                                const uint16_t* __restrict__ w1, const uint16_t* __restrict__ b1, const uint16_t* __restrict__ w2,
                                                const uint16_t* __restrict__ b2, char* ws, uint16_t* __restrict__ out) {
  extern __shared__ __attribute__((aligned(16))) char smem_[];
  LDS char* sm = (LDS char*)smem_;
  asm volatile("" : "+s"(x), "+s"(wr), "+s"(wi), "+s"(w1), "+s"(b1), "+s"(w2), "+s"(b2), "+s"(ws), "+s"(out));
  const int t = threadIdx.x, l = t & 63, wv = __builtin_amdgcn_readfirstlane(t >> 6), r = l & 15, h = l >> 4;
  const int bid = blockIdx.x;
  const int cb = (bid & 7) * 32 + (bid >> 3), g = cb >> 2, jb = cb & 3;
  const rsrc_t rw = mkrs(ws, WS_END);
  const f32x4 z4 = {0.f, 0.f, 0.f, 0.f};
  const u32x4 P4 = {POISON, POISON, POISON, POISON};
  const int srow0 = 512 * (g >> 4) + 256 * (g & 1) + 16 * ((g >> 3) & 1) + 4 * ((g >> 1) & 3);
  const int ft0 = 22 * jb + wv;
  const bool has3 = wv < 6 && ft0 + 16 < 86;
  const u32 tag = *(const CST u32*)(ws + O_EP) + 1u;
  const int i0q = (t * 21846) >> 16, i0p = t - 3 * i0q, i1q = ((t + 512) * 21846) >> 16, i1p = t + 512 - 3 * i1q;
  u32x4 xf[4]; u32 bv;

  auto w3dma = [&](int g0, int gs, auto n) {
    const rsrc_t r1 = mkrs(w1, D_ * F_ * 2);
    const u32 va = (l >> 3) * (F_ * 2) + ((l ^ (l >> 3 & 6)) & 7) * 16, vb = (l >> 2) * (F_ * 2) + ((l ^ (l >> 3 & 2)) & 3) * 16;
#pragma unroll
    for (int i = 0; i < n.v; ++i) {
      const int G = g0 + gs * i; const u32 so = G * (16 * F_ * 2) + 704 * jb + 512;
      dmap(r1, L_H + G * 3072, va, so); dmap(r1, L_H + G * 3072 + 1024, va, so + 8 * F_ * 2); dmap(r1, L_H + G * 3072 + 2048, vb, so + (jb == 3 ? 0 : 128));
    }
  };
  {
    const rsrc_t rx = mkrs(x, S_ * D_ * 2);
    u32x4 xa = ald4(rx, 64 * r + 16 * h, (256 * (bid >> 5) + 32 * wv + (bid & 31)) * (D_ * 2));
    const int ksr = ((bid & 63) + 64 * (8 * (bid >> 6) + wv)) * DF_;
    u32x2 wa = ald2(wr + ksr, 8 * l), wb = ald2(wi + ksr, 8 * l);
    u32 na = ((const CST u32*)wr)[(ksr >> 1) + 128], nb = ((const CST u32*)wi)[(ksr >> 1) + 128];
    SB();
    u32 v[8];
    const int id = (wv < 3 ? wv : wv - 3) * 256 + bid, tp = id >> 4;
    const bool t1 = wv < 3 && id < 688, t2 = wv >= 3 && id < 688, nat = tp - 11 * ((tp * 47) >> 9) >= 8;
    if (t1) {
      const rsrc_t rt = mkrs(w1, D_ * F_ * 2);
      const u32 so = (id & 15) * (64 * F_) + 64 * tp, vo = h * (8 * F_) + 4 * r;
#pragma unroll
      for (int j = 0; j < 8; ++j) v[j] = ald1(rt, vo, so + ((j >> 2) * 16 + (j & 3)) * (F_ * 2));
    } else if (t2) {
      const rsrc_t rt = mkrs(w2, D_ * F_ * 2);
      const int sh = nat ? 10 : 11;
      const u32 so = tp * 32768 + ((id & 8) << (nat ? 11 : 7)) + 128 * (id & 7), vo = ((l >> 5) << (sh + 3)) + 4 * (l & 31);
#pragma unroll
      for (int j = 0; j < 8; ++j) v[j] = ald1(rt, vo, so + (j << sh));
    } else if (wv == 7) {
      unsigned long long pc; u32 len;
      asm volatile("s_getpc_b64 %0\n.Lpf_a:\n\ts_mov_b32 %1, .Lpf_b-.Lpf_a" : "=s"(pc), "=s"(len));
      const rsrc_t rc = mkrs((const void*)pc, len);
#pragma unroll
      for (int i = 0; i < 3; ++i) touchp(rc, L_DUM, l * 128, i * 8192);
    }
    SB();
    {
      const int i = wv & 3, n2 = 4 * h + i;
      LDS char* cp = sm + L_CA + l * 16 + i * 4;
      *(LDS u32*)(cp + (L_CC - L_CA) + 8192 + (wv >> 2) * 1024) = (r >> 1) + 8 * (wv >> 2) == n2 ? 0x3F80u << (16 * (r & 1)) : 0u;
      if (wv < 4) {
        const float a0 = (float)((2 * n2 * r) & 31) * (1.f / 32.f), a1 = (float)(((2 * n2 + 1) * r) & 31) * (1.f / 32.f);
        *(LDS u32*)cp = pk(cosr(a0), cosr(a1));
        *(LDS u32*)(cp + 1024) = r == 0 ? 0xBF803F80u : pk(-sinr(a0), -sinr(a1));
        const float at = (float)(r * n2) * (1.f / 512.f);
        *(LDS float*)(cp + 2048) = cosr(at); *(LDS float*)(cp + 3072) = sinr(at);
      } else {
        const float ph = (float)((r * n2) & 15) * (1.f / 16.f), c = cosr(ph), sn = sinr(ph);
        *(LDS u32*)(cp + 4096) = pk(c, sn); *(LDS u32*)(cp + 5120) = pk(-sn, c) ^ (r >= 8 ? 0x80008000u : 0u);
        const float ps = (float)((r * n2) & 31) * (1.f / 32.f);
        const u32 cs = pk(cosr(ps), r == 0 ? ((i & 1) ? -1.f : 1.f) : -sinr(ps));
        *(LDS u32*)(cp + 6144) = (r & 1) ? cs << 16 : cs & 0xFFFFu;
        *(LDS u32*)(cp + 7168) = (r & 1) ? cs & 0xFFFF0000u : cs >> 16;
      }
    }
    const int k1 = (t >> 3) & 15, k2 = ((t >> 5) & 12) | ((t >> 1) & 3), ka = k1 + 32 * k2;
    LDS char* kp = sm + L_B1 + (t & 1) * 4160 + (k1 ? (wv < 4 ? ka : 512 - ka) : 16 * k2) * 4;
    PIN(kp);
    SB();
    auto xfld = [&]() {
#pragma unroll
      for (int i = 0; i < 4; ++i) xf[i] = ald4(rx, 64 * r + 16 * h, (srow0 + 32 * wv + i) * (D_ * 2));
    };
    if (PZ == 0) bst(P4, rw, t * 16, O_X2 + cb * PST);
    if (XP == 0) xfld();
    SB();
    constexpr int NV = (PZ == 0) + 4 * (XP == 0);
    asm volatile("s_cmp_eq_u32 %11, 1\n\ts_cbranch_scc1 1f\n\ts_cmp_eq_u32 %11, 2\n\ts_waitcnt vmcnt(%12)\n\ts_cbranch_scc1 1f\n\ts_waitcnt vmcnt(%13)\n1:\n\ts_waitcnt vmcnt(%14)"
                 : "+v"(xa), "+v"(wa), "+v"(wb), "+v"(v[0]), "+v"(v[1]), "+v"(v[2]), "+v"(v[3]), "+v"(v[4]), "+v"(v[5]), "+v"(v[6]), "+v"(v[7])
                 : "s"(__builtin_amdgcn_readfirstlane(t2 ? 1 : wv == 7 ? 2 : 0)), "n"(NV + 3), "n"(NV), "n"(NV + 8) : "scc", "memory");
    asm volatile("" : "+s"(na), "+s"(nb));
    ldsw(sm + L_B1 + wv * 1040 + l * 16, u32x4{__builtin_amdgcn_perm(wb[0], wa[0], 0x05040100u), __builtin_amdgcn_perm(wb[0], wa[0], 0x07060302u),
                                               __builtin_amdgcn_perm(wb[1], wa[1], 0x05040100u), __builtin_amdgcn_perm(wb[1], wa[1], 0x07060302u)});
    *(LDS u32*)(sm + L_B1 + wv * 1040 + 1024) = (ksr & 1) ? (na >> 16) | (nb & 0xFFFF0000u) : (na & 0xFFFFu) | (nb << 16);
    u32x4 p0, p1;
    auto split = [&]() {
#pragma unroll
      for (int i = 0; i < 4; ++i) { p0[i] = __builtin_amdgcn_perm(v[2 * i + 1], v[2 * i], 0x05040100u); p1[i] = __builtin_amdgcn_perm(v[2 * i + 1], v[2 * i], 0x07060302u); }
    };
    if (t1) {
      split();
      const u32 o = O_W1 + ((id >> 4) * 32 + (id & 15)) * 1024;
      if (!nat) { bst(p0, rw, l * 16, o); bst(p1, rw, l * 16, o + 16384); }
      const u32x4 ones = {0x3F803F80u, 0x3F803F80u, 0x3F803F80u, 0x3F803F80u};
      const float c0 = mm(ones, p0, z4)[0], c1 = mm(ones, p1, z4)[0];
      if (l < 32) __builtin_amdgcn_raw_buffer_store_b32(__builtin_bit_cast(u32, l < 16 ? c0 : c1), rw, nat ? 8 * r + 4 * h : l * 4, O_CB + (id & 15) * 5632 + (id >> 4) * 128, SC1);
    }
    bar();
    if (PZ == 1) bst(P4, rw, t * 16, O_X2 + cb * PST);
    const f32x4 d0 = mm(xa, ldsr(sm + L_CC + 8192 + l * 16), z4), d1 = mm(xa, ldsr(sm + L_CC + 9216 + l * 16), z4);
    { u32x4 q;
#pragma unroll
      for (int i = 0; i < 4; ++i) q[i] = *(LDS u32*)(kp + i * 1040);
      bst(q, rw, t * 16, O_WT + bid * PST); }
    if (PZ == 2) bst(P4, rw, t * 16, O_X2 + cb * PST);
    LDS char* cq = sm + L_CA + l * 16;
    const u32x4 C1 = ldsr(cq), S1 = ldsr(cq + 1024), R2 = ldsr(cq + 4096), I2 = ldsr(cq + 5120), SR = ldsr(cq + 6144), SI = ldsr(cq + 7168);
    const f32x4 tc = *(LDS f32x4*)(cq + 2048), ts = *(LDS f32x4*)(cq + 3072);
    if (wv == 7) {
      u32x4 q;
#pragma unroll
      for (int i = 0; i < 4; ++i) q[i] = *(LDS u32*)(sm + L_B1 + (4 * (t & 1) + i) * 1040 + 1024);
      if (l < 2) bst(q, rw, 8192 + l * 16, O_WT + bid * PST);
    }
    SB();
    if (XP == 1 || XP == 3) xfld();
    if (XP == 3) w3dma(wv, 8, IC<NDA>());
    SB();
    float s1 = 0.f, s2 = 0.f;
#pragma unroll
    for (int j = 0; j < 4; ++j) { s1 += d0[j] + d1[j]; s2 = __builtin_fmaf(d0[j], d0[j], __builtin_fmaf(d1[j], d1[j], s2)); }
    s1 = wsum(s1); s2 = wsum(s2);
    float mb, inv; lnstat(s1, s2, mb, inv);
    u32x4 a;
#pragma unroll
    for (int j = 0; j < 4; ++j) a[j] = pk((d0[j] - mb) * inv, (d1[j] - mb) * inv);
    f32x4 yr = mm(a, C1, z4), yi = mm(a, S1, z4);
    u32x4 b;
#pragma unroll
    for (int j = 0; j < 4; ++j) b[j] = pk(yr[j] * tc[j] + yi[j] * ts[j], yi[j] * tc[j] - yr[j] * ts[j]);
    f32x4 xr = mm(R2, b, z4), xi = mm(I2, b, z4), sr = mm(SR, b, z4), si = mm(SI, b, z4);
    u32x4 o;
#pragma unroll
    for (int j = 0; j < 4; ++j) o[j] = pk(r == 0 ? sr[j] : xr[j], r == 0 ? si[j] : xi[j]);
    ldsw(sm + L_HT + wv * 1040 + l * 16, o);
    if (t < 2) *(LDS u32*)(sm + L_FL + t * 4) = 0u;
    SB();
    if (XP == 4) xfld();
    asm volatile("s_waitcnt vmcnt(%0)" :: "n"(XP >= 3 ? 4 + 3 * NDA : 0) : "memory");
    bar();
    { u32x4 q; q[3] = tag;
#pragma unroll
      for (int j = 0; j < 3; ++j) q[j] = *(LDS u32*)(sm + L_HT + (3 * i0p + j) * 1040 + i0q * 4);
      bst(q, rw, t * 16, O_X1 + bid * PS1);
      if (wv < 4) {
#pragma unroll
        for (int j = 0; j < 3; ++j) q[j] = *(LDS u32*)(sm + L_HT + (3 * i1p + j) * 1040 + i1q * 4);
        bst(q, rw, t * 16 + 8192, O_X1 + bid * PS1);
      } }
    SB();
    if (t2) {
      int n = tp - 11 * ((id & 7) >> 1); n = n < 0 ? n + 43 : n;
      const u32 o2 = O_W2 + ((id & 7) * 176 + ((id >> 3) & 1) * 44 + n) * 1024;
#pragma unroll
      for (int i = 0; i < 8; ++i) PIN(v[i]);
      split();
      bst(p0, rw, l * 16, o2); bst(p1, rw, l * 16, o2 + 88 * 1024);
    }
    if (XP == 2) xfld();
    SB();
  }
  if (wv >= 4) {
    w3dma(8 * NDA + wv - 4, 4, IC<NDM>());
    const int i = wv & 3, m = 4 * h + i;
    LDS char* cp = sm + L_CC + l * 16 + i * 4;
    float a = (float)((r * m) & 15) * (1.f / 16.f), c = cosr(a), sn = sinr(a);
    const u32 sg = h >= 2 ? 0x80000000u : 0u;
    *(LDS u32*)cp = pk(c, -sn) ^ sg; *(LDS u32*)(cp + 1024) = pk(sn, c) ^ sg;
    *(LDS u32*)(cp + 4096) = m == 0 ? 0x3F803F80u : pk(2.f * c, -2.f * sn);
    a = (float)((r * m) & 31) * (1.f / 32.f);
    const u32 v = m == 0 ? ((r & 1) ? 0xBF803F80u : 0x3F803F80u) : pk(2.f * cosr(a), -2.f * sinr(a));
    *(LDS u32*)(cp + 2048) = (i & 1) ? 0u : v; *(LDS u32*)(cp + 3072) = (i & 1) ? v : 0u;
    a = (float)((m * (2 * r + 1)) & 31) * (1.f / 32.f);
    *(LDS u32*)(cp + 5120) = m == 0 ? 0xBF803F80u : pk(2.f * cosr(a), -2.f * sinr(a));
    a = (float)(r * m) * (1.f / 512.f);
    *(LDS float*)(cp + 6144) = cosr(a); *(LDS float*)(cp + 7168) = sinr(a);
  }
  SB();

  u32x4 wf[2][16];
  const u32 w1b = O_W1 + ft0 * 16384;
#define W1LD(e) wf[(e) & 1][(e) >> 1] = bld(rw, l * 16 + (((e) >> 1) & 3) * 1024, w1b + ((e) & 1) * 131072 + ((e) >> 3) * 4096)
  const int job = wv >> 2;
  LDS char* ap, *tp;
  u32x4 R, I, SA, SB_, M0, M1; f32x4 tc, ts, y0[4], y1[4];
  auto cpro = [&]() {
    LDS char* cq = sm + L_CC + l * 16;
    const u32x4 E0 = ldsr(cq + 8192), E1 = ldsr(cq + 9216);
    R = ldsr(cq); I = ldsr(cq + 1024); SA = ldsr(cq + 2048); SB_ = ldsr(cq + 3072); M0 = ldsr(cq + 4096); M1 = ldsr(cq + 5120);
    tc = *(LDS f32x4*)(cq + 6144); ts = *(LDS f32x4*)(cq + 7168);
#pragma unroll
    for (int i = 0; i < 4; ++i) { y0[i] = mm(E0, xf[i], z4); y1[i] = mm(E1, xf[i], z4); PIN(y0[i]); PIN(y1[i]); }
    PIN(tc); PIN(ts);
    ap = sm + L_HT + r * 1040 + h * 256;
    tp = sm + L_H + (job ? 2048 + (l >> 2) * 64 + ((wv ^ h) & 1) * 32 : (l >> 2) * 128 + ((wv ^ (l >> 3)) & 3) * 32) + (l & 3) * 8;
    PIN(ap); PIN(tp);
    PIN(R); PIN(I); PIN(SA); PIN(SB_); PIN(M0); PIN(M1);
    SB();
  };
  {
    const int w = wv & 3;
    const bool act = job == 0 || cb == 0;
    u32x4 wt[2], R1[4], I1[4], R2[2][2], I2[2][2];
    float tc[2][4], ts[2][4], uc[2][4], us[2][4];
    if (act) {
      int lo = l; PIN(lo);
      const int r = lo & 15, h = lo >> 4;
      const int k1 = 16 * w + r;
      const u32 odd = (r & 1) ? 0x80008000u : 0u, two = (r & 2) ? 0x80008000u : 0u;
#pragma unroll
      for (int i = 0; i < 4; ++i) {
        const int m = 4 * h + i;
        float a = (float)((k1 * m) & 63) * (1.f / 64.f), c = cosr(a), sn = sinr(a);
        u32 R = pk(c, sn), I = pk(-sn, c);
        R1[0][i] = R; I1[0][i] = I; R1[2][i] = R ^ odd; I1[2][i] = I ^ odd;
        u32 Rq = ((r & 1) ? I : R) ^ two, Iq = ((r & 1) ? R : I) ^ two ^ odd;
        R1[1][i] = Rq; I1[1][i] = Iq; R1[3][i] = Rq ^ odd; I1[3][i] = Iq ^ odd;
        a = (float)((r * m) & 31) * (1.f / 32.f); c = cosr(a); sn = sinr(a);
        R = pk(c, sn); I = pk(-sn, c);
        const u32 fi = (i & 1) ? 0x80008000u : 0u;
        R2[0][0][i] = R; I2[0][0][i] = I; R2[0][1][i] = R ^ odd; I2[0][1][i] = I ^ odd;
        R2[1][0][i] = R ^ fi; I2[1][0][i] = I ^ fi; R2[1][1][i] = R ^ odd ^ fi; I2[1][1][i] = I ^ odd ^ fi;
#pragma unroll
        for (int u = 0; u < 2; ++u) {
          a = (float)(k1 * (16 * u + m)) * (1.f / 2048.f); tc[u][i] = cosr(a) * 0x1p-20f; ts[u][i] = sinr(a) * 0x1p-20f;
          a = (float)((16 * w + m) * (16 * u + r)) * (1.f / 2048.f); uc[u][i] = cosr(a); us[u][i] = sinr(a);
          PIN(tc[u][i]); PIN(ts[u][i]); PIN(uc[u][i]); PIN(us[u][i]);
        }
      }
#pragma unroll
      for (int i = 0; i < 4; ++i) { PIN(R1[i]); PIN(I1[i]); PIN(R2[i >> 1][i & 1]); PIN(I2[i >> 1][i & 1]); }
    }
    u32 vt = ((h >> 1) * 64 + 16 * w + r) * PST + (h & 1) * 16, vb = umin(t >= 256 ? t : (t & ~31) + 2 * r + (h & 1), jb == 3 ? 319 : 351) * 2;
    PIN(vt); PIN(vb);
    SB();
    if (WP == 0) WAITV(0);
    {
      const int tt = t - 256, jq = ((tt + 512) * 21846) >> 16, jp = tt + 512 - 3 * jq;
      u32x4 g0, g1; u32 v0 = i0q * PS1 + i0p * 16, v1 = jq * PS1 + jp * 16;
      #pragma unroll 1
    for (int it = 0; it < SPIN; ++it) {
        asm volatile("" : "+v"(v0), "+v"(v1) :: "memory");
        g0 = bld(rw, v0, O_X1 + cb * 48);
        bool ok;
        if (job) { g1 = bld(rw, v1, O_X1 + cb * 48); ok = (g0[3] == tag) & (g1[3] == tag); } else ok = g0[3] == tag;
        if (all(ok)) break;
        __builtin_amdgcn_s_sleep(2);
      }
      WAITV(0);
      PIN(xf[0]); PIN(xf[1]); PIN(xf[2]); PIN(xf[3]);
      LDS char* p = sm + L_B0 + i0q * 32 + i0p * 12;
      *(LDS u32*)p = g0[0]; *(LDS u32*)(p + 4) = g0[1]; if (i0p != 2) *(LDS u32*)(p + 8) = g0[2];
      if (job) { p = sm + L_B0 + jq * 32 + jp * 12; *(LDS u32*)p = g1[0]; *(LDS u32*)(p + 4) = g1[1]; if (jp != 2) *(LDS u32*)(p + 8) = g1[2]; }
    }
    if (cb == 0) {
      bar();
      const u32x4 a = ldsr(sm + L_B0 + t * 16);
      ldsw(sm + L_B0 + t * 16, a & 0xFFFFu); ldsw(sm + L_HT + t * 16, a >> 16);
    }
    bar();
    if (act) {
#pragma unroll
      for (int u = 0; u < 2; ++u) wt[u] = bld(rw, vt + u * 128 * PST, O_WT + (job ? 8192 : cb * 32));
    } else __builtin_amdgcn_s_sleep(1);
    SB();
    bv = __builtin_amdgcn_raw_buffer_load_b16(mkrs(b1, F_ * 2), vb, 704 * jb, 0);
    SB();
    if (act) {
      u32x4 bf[2], af[2][4];
      LDS char* ap = sm + (job ? L_HT : L_B0) + ((h >> 1) * 32 + r) * 32 + (h & 1) * 16;
#pragma unroll
      for (int u = 0; u < 2; ++u)
#pragma unroll
        for (int q = 0; q < 4; ++q) af[u][q] = ldsr(ap + q * 2048 + u * 512);
      SB();
#pragma unroll
      for (int u = 0; u < 2; ++u) {
        f32x4 yr = z4, yi = z4;
#pragma unroll
        for (int q = 0; q < 4; ++q) { yr = mm(af[u][q], R1[q], yr); yi = mm(af[u][q], I1[q], yi); }
#pragma unroll
        for (int j = 0; j < 4; ++j) bf[u][j] = pk(yr[j] * tc[u][j] + yi[j] * ts[u][j], yi[j] * tc[u][j] - yr[j] * ts[u][j]);
      }
      u32x4 ef[2];
#pragma unroll
      for (int u = 0; u < 2; ++u) {
        f32x4 xr = mm(R2[u][1], bf[1], mm(R2[u][0], bf[0], z4)), xi = mm(I2[u][1], bf[1], mm(I2[u][0], bf[0], z4));
#pragma unroll
        for (int j = 0; j < 4; ++j) { float a = blo(wt[u][j]), b = bhi(wt[u][j]); ef[u][j] = pk(xr[j] * a - xi[j] * b, -(xr[j] * b + xi[j] * a)); }
      }
#pragma unroll
      for (int u = 0; u < 2; ++u) {
        f32x4 yr = mm(ef[1], R2[u][1], mm(ef[0], R2[u][0], z4)), yi = mm(ef[1], I2[u][1], mm(ef[0], I2[u][0], z4));
        u32x4 o;
#pragma unroll
        for (int j = 0; j < 4; ++j) o[j] = pk(yr[j] * uc[u][j] + yi[j] * us[u][j], yi[j] * uc[u][j] - yr[j] * us[u][j]);
        ldsw(sm + L_B1 + job * 8192 + ((u * 4 + w) * 64 + l) * 16, o);
      }
      if (l == 0) __hip_atomic_fetch_add((LDS u32*)(sm + L_FL + 4), 1u, __ATOMIC_RELAXED, __HIP_MEMORY_SCOPE_WORKGROUP);
    }
    if (job) {
#pragma unroll
      for (int c = 0; c < 6; ++c) if (w + 4 * c < 22) bst(P4, rw, l * 16, O_HB + (g * 88 + 22 * jb + w + 4 * c) * 1024);
      w3dma(8 * NDA + 4 * NDM + w, 4, IC<8 - 2 * NDA - NDM>());
      const int tt = t - 256;
      u32x4 cp[8];
      if (w < 3) {
#pragma unroll
        for (int i = 0; i < 8; ++i) cp[i] = bld(rw, ((tt & 1) * 8 + i) * 5632 + (tt >> 1) * 16, O_CB + jb * 1408);
      }
      SB();
      if (PM == 1) cpro();
#pragma unroll
      for (int e = 0; e < NJ1; ++e) W1LD(e);
      SB();
      if (PM == 2) cpro();
      if (w < 3) {
        f32x4 cs = z4;
#pragma unroll
        for (int i = 0; i < 8; ++i) cs += __builtin_bit_cast(f32x4, cp[i]);
#pragma unroll
        for (int j = 0; j < 4; ++j) cs[j] += dpp<0xB1>(cs[j]);
        if (tt < 176) *(LDS f32x4*)(sm + L_CS + (tt >> 1) * 16) = cs;
      }
      SB();
#pragma unroll
      for (int e = NJ1; e < NJ2; ++e) W1LD(e);
    } else {
#pragma unroll 1
      for (int it = 0; it < SPIN; ++it) {
        asm volatile("" ::: "memory");
        if (__builtin_amdgcn_readfirstlane(*(volatile LDS u32*)(sm + L_FL + 4)) == (cb ? 4 : 8)) break;
      }
      asm volatile("" ::: "memory");
      u32x4 af[2][4];
#pragma unroll
      for (int u = 0; u < 2; ++u)
#pragma unroll
        for (int q = 0; q < 4; ++q) af[u][q] = ldsr(sm + L_B1 + ((u * 4 + q) * 64 + l) * 16);
      if (cb != 0) {
#pragma unroll
        for (int u = 0; u < 2; ++u) {
          f32x4 zr = z4, zi = z4;
#pragma unroll
          for (int q = 0; q < 4; ++q) { zr = mm(af[u][q], R1[q], zr); zi = mm(af[u][q], I1[q], zi); }
          u32x4 o;
#pragma unroll
          for (int j = 0; j < 4; ++j) o[j] = umin(pk(zr[j], -zi[j]), POISON - 1);
          bst(o, rw, ((w * 2 + u) * 64 + l) * 16, O_X2 + cb * PST);
        }
      } else {
#pragma unroll
        for (int u = 0; u < 2; ++u) {
          f32x4 zr = z4, zi = z4;
#pragma unroll
          for (int q = 0; q < 4; ++q) { zr = mm(af[u][q], R1[q], zr); zi = mm(ldsr(sm + L_B1 + 8192 + ((u * 4 + q) * 64 + l) * 16), R1[q], zi); }
          u32x4 o;
#pragma unroll
          for (int j = 0; j < 4; ++j) o[j] = umin(pk(zr[j], zi[j]), POISON - 1);
          bst(o, rw, ((w * 2 + u) * 64 + l) * 16, O_X2);
        }
        if (t == 0) __builtin_amdgcn_raw_buffer_store_b32(tag, rw, 0, O_EP, SC1);
      }
      SB();
#pragma unroll
      for (int e = 0; e < NW1A; ++e) W1LD(e);
      SB();
      if (PM) cpro();
    }
    SB();
  }

  {
    if (!PM) cpro();
    if (t < 352) *(LDS u32*)(sm + L_BB + t * 4) = bv << 16;
    SB();
    asm volatile("s_waitcnt vmcnt(%0)" :: "n"(WQ ? NJ2 : 0) : "memory");
    bar();
    if (t == 0) __builtin_amdgcn_raw_buffer_store_b32(tag, rw, 0, O_FL + cb * 128, SC1);
    u32x4 zin[4]; u32 vz = (t >> 3) * PST + (t & 7) * 16;
    auto zld = [&]() {
      asm volatile("" : "+v"(vz) :: "memory");
#pragma unroll
      for (int i = 0; i < 4; ++i) zin[i] = bld(rw, vz, O_X2 + i * 64 * PST + g * 128);
    };
    auto zok = [&]() {
      u32x4 m;
#pragma unroll
      for (int j = 0; j < 4; ++j) m[j] = umax(umax(umax(zin[j][0], zin[j][1]), zin[j][2]), zin[j][3]);
      return all(clean(m));
    };
    zld();
    SB();
    if (PB) asm volatile("s_barrier" ::: "memory");
#pragma unroll
    for (int e = NJ2; e < NJ2 + NPA; ++e) W1LD(e);
    SB();
    if (!zok()) {
#pragma unroll 1
      for (int it = 0; it < SPIN; ++it) {
        zld();
        if (zok()) break;
        __builtin_amdgcn_s_sleep(2);
      }
    }
#pragma unroll
    for (int i = 0; i < 4; ++i) ldsw(sm + L_HT + (t & 7) * 4160 + ((t >> 3) & 3) * 1024 + ((16 * i + (t >> 5) + ((t & 7) >> 1)) & 63) * 16, zin[i]);
    bar();
    u32x4 q[4], zs;
#pragma unroll
    for (int j = 0; j < 4; ++j) {
      q[j] = ldsr(sm + L_HT + wv * 4160 + j * 1024 + ((l + (wv >> 1)) & 63) * 16);
      zs[j] = *(LDS u32*)(sm + L_HT + wv * 4160 + j * 1024 + (16 * h + (wv >> 1)) * 16 + (r & 3) * 4);
    }
    {
      f32x4 ur[4], ui[4];
      const f32x4 sa = mm(zs, SA, z4), sb = mm(zs, SB_, z4);
      { const u32x4 z = {q[0][0], q[1][0], q[2][0], q[3][0]}; ur[0] = mm(z, R, z4); ui[0] = mm(z, I, z4); }
#pragma unroll
      for (int i = 0; i < 4; ++i) {
        if (i < 3) { const u32x4 z = {q[0][i + 1], q[1][i + 1], q[2][i + 1], q[3][i + 1]}; ur[i + 1] = mm(z, R, z4); ui[i + 1] = mm(z, I, z4); }
        SB();
        if (!job) {
#pragma unroll
          for (int e = NW1A + (NJ2 - NW1A) * i / 4; e < NW1A + (NJ2 - NW1A) * (i + 1) / 4; ++e) W1LD(e);
        }
#pragma unroll
        for (int e = NJ2 + NPA + (32 - NJ2 - NPA) * i / 4; e < NJ2 + NPA + (32 - NJ2 - NPA) * (i + 1) / 4; ++e) W1LD(e);
        ur[i][0] = h == 0 ? sa[i] : ur[i][0]; ui[i][0] = h == 0 ? sb[i] : ui[i][0];
        u32x4 v;
#pragma unroll
        for (int j = 0; j < 4; ++j) v[j] = pk(ur[i][j] * tc[j] - ui[i][j] * ts[j], ui[i][j] * tc[j] + ur[i][j] * ts[j]);
        y0[i] = mm(v, M0, y0[i]); y1[i] = mm(v, M1, y1[i]);
        if (i) ldsw(sm + L_HT + (4 * wv + i - 1) * 1040 + l * 16, u32x4{pk(y0[i - 1][0], y0[i - 1][1]), pk(y0[i - 1][2], y0[i - 1][3]), pk(y1[i - 1][0], y1[i - 1][1]), pk(y1[i - 1][2], y1[i - 1][3])});
      }
      ldsw(sm + L_HT + (4 * wv + 3) * 1040 + l * 16, u32x4{pk(y0[3][0], y0[3][1]), pk(y0[3][2], y0[3][3]), pk(y1[3][0], y1[3][1]), pk(y1[3][2], y1[3][3])});
    }
    bar();
  }

  const int cg = wv & 3, kh = wv >> 2;
  u32x4 w2f[43];
  u32 vf = (l & 3) * 128, fl = __builtin_amdgcn_raw_buffer_load_b32(rw, vf, O_FL + g * 512, SC1);
  const u32x4 bq = *(const GLB u32x4*)((const GLB uint16_t*)b2 + 128 * jb + 8 * (t & 15));
  const u32 w2b = O_W2 + ((4 * jb + cg) * 88 + kh * 44) * 1024;
#define W2LD(i) w2f[i] = bld(rw, l * 16 + ((i) & 3) * 1024, w2b + ((i) >> 2) * 4096)
  {
    f32x4 acc[3][2];
#pragma unroll
    for (int c = 2; c < 3; ++c) { acc[c][0] = z4; acc[c][1] = z4; PIN(acc[c][0]); PIN(acc[c][1]); }
    {
      const bool gm = wv >= 6;
      if (gm) __builtin_amdgcn_s_setprio(1);
      if (gm) {
        const u32x4 ones = {0x3F803F80u, 0x3F803F80u, 0x3F803F80u, 0x3F803F80u};
        LDS char* gp = ap + (wv & 1) * 16640;
        u32x4 q[8]; f32x4 s1 = z4, s2 = z4;
#pragma unroll
        for (int i = 0; i < 8; ++i) q[i] = ldsr(gp + i * 16);
#pragma unroll
        for (int i = 0; i < 16; ++i) { s2 = mm(q[i & 7], q[i & 7], s2); s1 = mm(ones, q[i & 7], s1); SB(); if (i < 8) q[i] = ldsr(gp + (i + 8) * 16); SB(); }
        const int e = r & 3;
        float mb, inv; lnstat(s1[0], e == 0 ? s2[0] : e == 1 ? s2[1] : e == 2 ? s2[2] : s2[3], mb, inv);
        if (h == (r >> 2)) *(LDS f32x2*)(sm + L_ST + ((wv - 6) * 16 + r) * 8) = f32x2{mb, inv};
        bar();
      }
      u32x4 a0[2][2], a1[2][2]; u32x2 w3[2][4];
#pragma unroll
      for (int u = 0; u < 2; ++u) { a0[0][u] = ldsr(ap + u * 16); a1[0][u] = ldsr(ap + 16640 + u * 16); }
#pragma unroll
      for (int u = 0; u < 4; ++u) w3[0][u] = ldst(tp + u * 3072);
      SB();
#pragma unroll
      for (int kk = 0; kk < 8; ++kk) {
        const int p = kk & 1;
        if (kk == PRK) __builtin_amdgcn_s_setprio(0);
#pragma unroll
        for (int u = 0; u < 2; ++u)
#pragma unroll
          for (int c = 0; c < 2; ++c) {
            mma(acc[c][0], wf[c][2 * kk + u], a0[p][u], kk + u == 0);
            SB();
            if (kk < 7) { if (u == 0) a0[p ^ 1][c] = ldsr(ap + (2 * kk + 2 + c) * 16); else w3[p ^ 1][2 * c] = ldst(tp + (4 * kk + 4 + 2 * c) * 3072); }
            SB();
            mma(acc[c][1], wf[c][2 * kk + u], a1[p][u], kk + u == 0);
            SB();
            if (kk < 7) { if (u == 0) a1[p ^ 1][c] = ldsr(ap + 16640 + (2 * kk + 2 + c) * 16); else w3[p ^ 1][2 * c + 1] = ldst(tp + (4 * kk + 5 + 2 * c) * 3072); }
#pragma unroll
            for (int i = NW2A * (4 * kk + 2 * u + c) / 32; i < NW2A * (4 * kk + 2 * u + c + 1) / 32; ++i) W2LD(i);
            SB();
          }
        if (!gm) {
#pragma unroll
          for (int u = 0; u < 2; ++u) {
            const u32x4 w = {w3[p][2 * u][0], w3[p][2 * u][1], w3[p][2 * u + 1][0], w3[p][2 * u + 1][1]};
            mma(acc[2][0], w, a0[p][u], false); mma(acc[2][1], w, a1[p][u], false);
          }
          if (kk == 2) asm volatile("s_barrier" ::: "memory");
        }
        SB();
      }
      asm volatile("s_nop 7\n\ts_nop 7" : "+v"(acc[0][0]), "+v"(acc[0][1]), "+v"(acc[1][0]), "+v"(acc[1][1]), "+v"(acc[2][0]), "+v"(acc[2][1]));
    }
    if (!all(fl == tag)) {
#pragma unroll 1
      for (int it = 0; it < SPIN; ++it) { asm volatile("" : "+v"(vf) :: "memory"); if (all(__builtin_amdgcn_raw_buffer_load_b32(rw, vf, O_FL + g * 512, SC1) == tag)) break; }
    }
    const f32x2 st0 = *(LDS f32x2*)(sm + L_ST + r * 8), st1 = *(LDS f32x2*)(sm + L_ST + (16 + r) * 8);
#pragma unroll
    for (int c = 0; c < 3; ++c) {
      if (c < 2 || has3) {
        const int ft = ft0 + 8 * c;
        const f32x4 cs = *(LDS f32x4*)(sm + L_CS + (wv + 8 * c) * 64 + h * 16), bb = *(LDS f32x4*)(sm + L_BB + (wv + 8 * c) * 64 + h * 16);
        float v0[4], v1[4];
#pragma unroll
        for (int j = 0; j < 4; ++j) {
          float p0 = __builtin_fmaxf((acc[c][0][j] - st0[0] * cs[j]) * st0[1] + bb[j], 0.f), p1 = __builtin_fmaxf((acc[c][1][j] - st1[0] * cs[j]) * st1[1] + bb[j], 0.f);
          v0[j] = p0 * p0; v1[j] = p1 * p1;
        }
        auto s0 = __builtin_amdgcn_permlane16_swap(pk(v0[0], v0[1]), pk(v1[0], v1[1]), false, false);
        auto s1 = __builtin_amdgcn_permlane16_swap(pk(v0[2], v0[3]), pk(v1[2], v1[3]), false, false);
        const u32x4 o = {(u32)s0[0], (u32)s1[0], (u32)s0[1], (u32)s1[1]};
        bst(o, rw, l * 16, O_HB + (g * 88 + ft) * 1024);
        ldsw(sm + L_H + (wv + 8 * c) * 1024 + l * 16, o);
      }
    }
  }

  {
    SB();
    if (!kh) {
#pragma unroll
      for (int i = NW2A; i < NW2B; ++i) W2LD(i);
    }
#pragma unroll
    for (int i = 0; i < 10; ++i) PIN(w2f[i]);
    SB();
    const u32 hsrc = O_HB + (g * 88 + wv) * 1024;
    auto hdma = [&]() {
#pragma unroll
      for (int m = 1; m < 4; ++m) {
        const int pj = (jb + m) & 3; const u32 sl = L_H + (22 * (pj - jb) + (pj < jb ? 86 : 0) + wv) * 1024;
        dma(rw, sl, l * 16, hsrc + pj * 22528); dma(rw, sl + 8192, l * 16, hsrc + pj * 22528 + 8192);
        if (wv < (pj == 3 ? 4 : 6)) dma(rw, sl + 16384, l * 16, hsrc + pj * 22528 + 16384);
      }
    };
    LDS char* hb = sm + L_H + kh * 1024 + l * 16;
    const int tok = t >> 4, e = t & 15, d0 = 128 * jb + 8 * e;
    LDS char* hp = sm + L_HT + tok * 1040 + (((d0 >> 2) & 3) * 16 + (d0 >> 5)) * 16 + ((d0 >> 4) & 1) * 8;
    const u32x2 h0 = *(LDS u32x2*)hp, h1 = *(LDS u32x2*)(hp + 256);
    bar();
    if (!kh) hdma();
    f32x16 acc = {0.f};
    float res[8];
    u32x4 hq[4];
#pragma unroll
    for (int i = 0; i < 3; ++i) hq[i] = ldsr(hb + i * 2048);
#pragma unroll
    for (int i = 0; i < 10; ++i) { if (i + 3 < 10) hq[(i + 3) & 3] = ldsr(hb + (i + 3) * 2048); SB(); acc = mm32(w2f[i], hq[i & 3], acc); SB(); }
#pragma unroll
    for (int i = 0; i < 4; ++i) { const u32 hv = i < 2 ? h0[i & 1] : h1[i & 1]; res[2 * i] = blo(bq[i]) + blo(hv); res[2 * i + 1] = bhi(bq[i]) + bhi(hv); PIN(res[2 * i]); PIN(res[2 * i + 1]); }
    LDS char* wp = sm + L_RED + (kh * 32 + (l & 31)) * 528 + (32 * cg + 4 * (l >> 5)) * 4;
    LDS char* rp = sm + L_RED + tok * 528 + (e >> 3) * 256 + (e & 7) * 16;
    u32 vo = ((srow0 + 32 * (tok >> 2) + (tok & 3)) * D_ + d0) * 2;
    LDS char* cp[3];
#pragma unroll
    for (int m = 1; m < 4; ++m) {
      const int pj = (jb + m) & 3;
      cp[m - 1] = sm + L_H + (22 * (pj - jb) + (pj < jb ? 86 : 0) + wv) * 1024 + umin(h, wv < (pj == 3 ? 4 : 6) ? 2 : 1) * 8192 + r * 64;
      PIN(cp[m - 1]);
    }
    PIN(wp); PIN(rp); PIN(vo);
    SB();
    if (kh) {
      WAITV(0);
      hdma();
#pragma unroll
      for (int i = NW2A; i < NW2C; ++i) W2LD(i);
      asm volatile("s_waitcnt vmcnt(%0)" :: "n"(NW2C - NW2A) : "memory");
    } else {
      if (SLP) __builtin_amdgcn_s_sleep(SLP);
#pragma unroll
      for (int i = NW2B; i < NW2D; ++i) W2LD(i);
      asm volatile("s_waitcnt vmcnt(%0)" :: "n"(NW2D - NW2B) : "memory");
    }
#pragma unroll 1
    for (int it = 0; it < SPIN; ++it) {
      if (all(umax(umax(*(LDS u32*)cp[0], *(LDS u32*)cp[1]), *(LDS u32*)cp[2]) != POISON)) break;
      hdma();
      WAITV(0);
    }
    bar();
#pragma unroll
    for (int i = 10; i < 13; ++i) hq[i & 3] = ldsr(hb + i * 2048);
#pragma unroll
    for (int i = 10; i < 43; ++i) {
      if (i + 3 < 43) hq[(i + 3) & 3] = ldsr(hb + (i + 3) * 2048);
      const int j = NW2C + i - 10;
      if (j < NW2D) { if ((i - 10) % 2 == 0 && kh) { W2LD(j); if (j + 1 < NW2D) W2LD(j + 1); } } else if (j < 43) W2LD(j);
      SB();
      acc = mm32(w2f[i], hq[i & 3], acc);
      SB();
    }
#pragma unroll
    for (int q = 0; q < 4; ++q) *(LDS f32x4*)(wp + 32 * q) = f32x4{acc[4 * q], acc[4 * q + 1], acc[4 * q + 2], acc[4 * q + 3]};
    if (acc[0] != acc[0]) *(LDS u32*)(sm + L_FL) = 1;
    bar();
    f32x4 a0 = *(LDS f32x4*)rp + *(LDS f32x4*)(rp + 32 * 528), a1 = *(LDS f32x4*)(rp + 128) + *(LDS f32x4*)(rp + 32 * 528 + 128);
    if (__builtin_amdgcn_readfirstlane(*(LDS u32*)(sm + L_FL))) {
#pragma unroll 1
      for (int f = wv; f < 86; f += 8) {
        int ft = f + 22 * jb; ft = ft >= 86 ? ft - 86 : ft;
        u32x4 q; u32 vh = l * 16;
#pragma unroll 1
        for (int it = 0; it < SPIN; ++it) {
          asm volatile("" : "+v"(vh) :: "memory");
          q = bld(rw, vh, O_HB + (g * 88 + ft) * 1024);
          if (all(clean(q))) break;
        }
        ldsw(sm + L_H + f * 1024 + l * 16, q);
      }
      bar();
      acc = f32x16{0.f};
#pragma unroll 1
      for (int i = 0; i < 43; ++i) acc = mm32(bld(rw, l * 16, w2b + i * 1024), ldsr(hb + i * 2048), acc);
#pragma unroll
      for (int q = 0; q < 4; ++q) *(LDS f32x4*)(wp + 32 * q) = f32x4{acc[4 * q], acc[4 * q + 1], acc[4 * q + 2], acc[4 * q + 3]};
      bar();
      a0 = *(LDS f32x4*)rp + *(LDS f32x4*)(rp + 32 * 528); a1 = *(LDS f32x4*)(rp + 128) + *(LDS f32x4*)(rp + 32 * 528 + 128);
    }
    u32x4 o;
#pragma unroll
    for (int i = 0; i < 4; ++i) o[i] = pk(a0[i] + res[2 * i], a1[i] + res[2 * i + 1]);
    __builtin_amdgcn_raw_buffer_store_b128(o, mkrs(out, S_ * D_ * 2), vo, 0, SC1);
  }
  asm volatile(".Lpf_b:");
}

#ifdef __HIP_DEVICE_COMPILE__
#pragma clang attribute pop
#endif
extern "C" void kernel_launch(void* const* d_in, const int* in_sizes, int n_in, void* d_out, int out_size, void* d_ws, size_t ws_size, hipStream_t stream) {
  (void)hipFuncSetAttribute((const void*)k_all, hipFuncAttributeMaxDynamicSharedMemorySize, L_END);
  k_all<<<256, 512, L_END, stream>>>((const uint16_t*)d_in[0], (const uint16_t*)d_in[1], (const uint16_t*)d_in[2], (const uint16_t*)d_in[4], (const uint16_t*)d_in[5],
                                     (const uint16_t*)d_in[6], (const uint16_t*)d_in[7], (char*)d_ws, (uint16_t*)d_out);
}
```
